```python
import torch

def _softmax(x, axis=-1):
    return torch.softmax(x, dim=axis)
def _gelu(x):
    return torch.nn.functional.gelu(x, approximate="tanh")
def _silu(x):
    return torch.nn.functional.silu(x)
def _sigmoid(x):
    return torch.sigmoid(x)
def _layernorm(x, axis=-1, eps=1e-5):
    m = x.mean(dim=axis, keepdim=True)
    v = x.var(dim=axis, keepdim=True, unbiased=False)
    return (x - m) / torch.sqrt(v + eps)
def _scatter_add(tgt, idx, src, axis=0):
    return tgt.clone().index_add_(axis, idx, src)
def _segment_sum(src, seg, n):
    out = torch.zeros((n,) + src.shape[1:], dtype=src.dtype)
    return out.index_add_(0, seg, src)
def _pad(x, pw):
    flat = tuple(v for pair in reversed(pw) for v in pair)
    return torch.nn.functional.pad(x, flat)

kernel_name = 'sequential_preLN__gqa_block_cached__swiglu_ffn__decode'

def setup_inputs():

    _w = (lambda a: (lambda t: t.to(torch.bfloat16) if t.is_floating_point() else t)(torch.as_tensor(a)))
    def _A_setup():
        B, Sq, D = 4, 1, 512
        Hq, Hkv, Sk, Dh = 32, 8, 4096, 128
        assert Hq % Hkv == 0, (Hq, Hkv)
        return {
            "x": _w((lambda *s: torch.randn(*s, dtype=torch.bfloat16) * 0.05)(B, Sq, D)),
            "k_cache": _w((lambda *s: torch.randn(*s, dtype=torch.bfloat16) * 3.2)(B, Hkv, Sk, Dh)),
            "v_cache": _w((lambda *s: torch.randn(*s, dtype=torch.bfloat16) * 0.4)(B, Hkv, Sk, Dh)),
            "wq": _w((lambda *s: torch.randn(*s, dtype=torch.bfloat16) * 3.2)(D, Hq * Dh) * (D ** -0.5)),
            "wo": _w((lambda *s: torch.randn(*s, dtype=torch.bfloat16) * 0.4)(Hq * Dh, D) * ((Hq * Dh) ** -0.5)),
        }
    _A_in = _A_setup()

    def _B_setup():
        B, S, D, F = 4, 1, 512, 1792
        return {
            "x": _w((lambda *s: torch.randn(*s, dtype=torch.bfloat16) * 0.05)(B, S, D)),
            "w_gate": _w((lambda *s: torch.randn(*s, dtype=torch.bfloat16) * 0.05)(D, F)),
            "w_up": _w((lambda *s: torch.randn(*s, dtype=torch.bfloat16) * 0.05)(D, F)),
            "w_down": _w((lambda *s: torch.randn(*s, dtype=torch.bfloat16) * 0.05)(F, D)),
        }
    _B_in = _B_setup()

    return {
        "A_x": _A_in["x"],
        "A_k_cache": _A_in["k_cache"],
        "A_v_cache": _A_in["v_cache"],
        "A_wq": _A_in["wq"],
        "A_wo": _A_in["wo"],
        "B_x": _B_in["x"],
        "B_w_gate": _B_in["w_gate"],
        "B_w_up": _B_in["w_up"],
        "B_w_down": _B_in["w_down"],
    }

def reference(A_x, A_k_cache, A_v_cache, A_wq, A_wo, B_x, B_w_gate, B_w_up, B_w_down):
    def _A_ref(x, k_cache, v_cache, wq, wo):
        B, Sq, D = x.shape
        Hq, Dh = 32, 128
        Hkv = k_cache.shape[1]
        rep = Hq // Hkv
        q = (lambda x, *d: x.permute(*d))((x @ wq).reshape(B, Sq, Hq, Dh), 0, 2, 1, 3)
        k = (lambda x, n, axis: torch.repeat_interleave(x, n, dim=axis))(k_cache, rep, axis=1)
        v = (lambda x, n, axis: torch.repeat_interleave(x, n, dim=axis))(v_cache, rep, axis=1)
        scores = torch.einsum("bhqd,bhkd->bhqk", q, k) * (Dh ** -0.5)
        p = _softmax(scores, axis=-1)
        o = torch.einsum("bhqk,bhkd->bhqd", p, v)
        return (lambda x, *d: x.permute(*d))(o, 0, 2, 1, 3).reshape(B, Sq, Hq * Dh) @ wo

    def _B_ref(x, w_gate, w_up, w_down):
        g = _silu(x @ w_gate)
        u = x @ w_up
        return (g * u) @ w_down

    x = A_x
    h = x + _A_ref(_layernorm(x, axis=-1), A_k_cache, A_v_cache, A_wq, A_wo)
    h2 = h + _B_ref(_layernorm(h, axis=-1), B_w_gate, B_w_up, B_w_down)
    return h2
```

```cpp
#include <hip/hip_runtime.h>
#include <hip/hip_bf16.h>
#include <stdint.h>

typedef unsigned short u16;
typedef unsigned v4u __attribute__((ext_vector_type(4)));

constexpr int B = 4, D = 512, HQ = 32, HKV = 8, SK = 4096, DH = 128, F = 1792;
constexpr int NSPLIT = 8, KPS = SK / NSPLIT;
#define MIMIC 1

__device__ __forceinline__ float bf2f(u16 v) { return __builtin_bit_cast(float, (unsigned)v << 16); }
__device__ __forceinline__ u16 f2bf(float f) { __bf16 b = (__bf16)f; return __builtin_bit_cast(u16, b); }
__device__ __forceinline__ float rbf(float f) {
  return bf2f(f2bf(f));
}

__device__ float block_sum(float v, float* red) {
  for (int o = 32; o; o >>= 1) v += __shfl_xor(v, o);
  int w = threadIdx.x >> 6;
  __syncthreads();
  if ((threadIdx.x & 63) == 0) red[w] = v;
  __syncthreads();
  float s = 0.f;
  for (int i = 0; i < (int)(blockDim.x >> 6); ++i) s += red[i];
  return s;
}

__device__ void ln_rows(float* xs, float* red) {
  for (int b = 0; b < B; ++b) {
    float s = 0.f;
    for (int i = threadIdx.x; i < D; i += blockDim.x) s += xs[b * D + i];
    float mean = block_sum(s, red) * (1.0f / D);
    float s2 = 0.f;
    for (int i = threadIdx.x; i < D; i += blockDim.x) { float d = xs[b * D + i] - mean; s2 += d * d; }
    float var = block_sum(s2, red) * (1.0f / D);
    float m = rbf(mean), v = rbf(var);
    float sd = rbf(sqrtf(rbf(v + 1e-5f)));
    __syncthreads();
    for (int i = threadIdx.x; i < D; i += blockDim.x) xs[b * D + i] = rbf(rbf(xs[b * D + i] - m) / sd);
    __syncthreads();
  }
}

__global__ void __launch_bounds__(256) k_qproj(const u16* x, const u16* wq, float* q) {
  __shared__ float xs[B * D];
  __shared__ float red[8];
  for (int i = threadIdx.x; i < B * D; i += 256) xs[i] = bf2f(x[i]);
  __syncthreads();
  ln_rows(xs, red);
  int n = blockIdx.x * 256 + threadIdx.x;
  float acc[B] = {0, 0, 0, 0};
  for (int k = 0; k < D; ++k) {
    float w = bf2f(wq[(size_t)k * (HQ * DH) + n]);
    for (int b = 0; b < B; ++b) acc[b] += xs[b * D + k] * w;
  }
  for (int b = 0; b < B; ++b) q[b * HQ * DH + n] = rbf(acc[b]);
}

__global__ void __launch_bounds__(256) k_attn(const float* q, const u16* kc, const u16* vc, float* po, float* pml) {
  __shared__ float qs[4 * DH];
  __shared__ float ps[4 * KPS];
  __shared__ float red[8];
  int s = blockIdx.x, kvh = blockIdx.y, b = blockIdx.z;
  for (int i = threadIdx.x; i < 4 * DH; i += 256) qs[i] = q[b * HQ * DH + kvh * 4 * DH + i];
  __syncthreads();
  const u16* kb = kc + ((size_t)(b * HKV + kvh) * SK + s * KPS) * DH;
  const u16* vb = vc + ((size_t)(b * HKV + kvh) * SK + s * KPS) * DH;
  for (int key = threadIdx.x; key < KPS; key += 256) {
    float a[4] = {0, 0, 0, 0};
    for (int d = 0; d < DH; d += 8) {
      v4u kk = *(const v4u*)(kb + (size_t)key * DH + d);
      for (int j = 0; j < 4; ++j) {
        float lo = __builtin_bit_cast(float, kk[j] << 16), hi = __builtin_bit_cast(float, kk[j] & 0xffff0000u);
        for (int h = 0; h < 4; ++h) a[h] += lo * qs[h * DH + d + 2 * j] + hi * qs[h * DH + d + 2 * j + 1];
      }
    }
    for (int h = 0; h < 4; ++h) ps[h * KPS + key] = rbf(rbf(a[h]) * 0.08838834764831845f);
  }
  __syncthreads();
  for (int h = 0; h < 4; ++h) {
    float m = -INFINITY;
    for (int i = threadIdx.x; i < KPS; i += 256) m = fmaxf(m, ps[h * KPS + i]);
    for (int o = 32; o; o >>= 1) m = fmaxf(m, __shfl_xor(m, o));
    __syncthreads();
    if ((threadIdx.x & 63) == 0) red[threadIdx.x >> 6] = m;
    __syncthreads();
    m = fmaxf(fmaxf(red[0], red[1]), fmaxf(red[2], red[3]));
    float l = 0.f;
    for (int i = threadIdx.x; i < KPS; i += 256) { float p = __expf(ps[h * KPS + i] - m); ps[h * KPS + i] = p; l += p; }
    l = block_sum(l, red);
    if (threadIdx.x == 0) { size_t r = ((size_t)(b * HQ + kvh * 4 + h) * NSPLIT + s); pml[2 * r] = m; pml[2 * r + 1] = l; }
  }
  __syncthreads();
  int d = threadIdx.x & 127, hp = threadIdx.x >> 7;
  float o0 = 0.f, o1 = 0.f;
  for (int key = 0; key < KPS; ++key) {
    float v = bf2f(vb[(size_t)key * DH + d]);
    o0 += ps[(2 * hp) * KPS + key] * v;
    o1 += ps[(2 * hp + 1) * KPS + key] * v;
  }
  po[((size_t)(b * HQ + kvh * 4 + 2 * hp) * NSPLIT + s) * DH + d] = o0;
  po[((size_t)(b * HQ + kvh * 4 + 2 * hp + 1) * NSPLIT + s) * DH + d] = o1;
}

__global__ void __launch_bounds__(128) k_combine(const float* po, const float* pml, float* o) {
  int r = blockIdx.x, d = threadIdx.x;
  float M = -INFINITY;
  for (int s = 0; s < NSPLIT; ++s) M = fmaxf(M, pml[2 * (r * NSPLIT + s)]);
  float L = 0.f, acc = 0.f;
  for (int s = 0; s < NSPLIT; ++s) {
    float w = __expf(pml[2 * (r * NSPLIT + s)] - M);
    L += w * pml[2 * (r * NSPLIT + s) + 1];
    acc += w * po[(size_t)(r * NSPLIT + s) * DH + d];
  }
  o[r * DH + d] = rbf(acc / L);
}

__global__ void __launch_bounds__(256) k_oproj(const float* o, const u16* wo, float* part) {
  int n = blockIdx.x * 64 + (threadIdx.x & 63), kg = threadIdx.x >> 6, ks = blockIdx.y;
  __shared__ float red[4][B][64];
  float acc[B] = {0, 0, 0, 0};
  for (int k = ks * 128 + kg * 32; k < ks * 128 + kg * 32 + 32; ++k) {
    float w = bf2f(wo[(size_t)k * D + n]);
    for (int b = 0; b < B; ++b) acc[b] += o[b * HQ * DH + k] * w;
  }
  for (int b = 0; b < B; ++b) red[kg][b][threadIdx.x & 63] = acc[b];
  __syncthreads();
  int b = kg, c = threadIdx.x & 63;
  part[(ks * B + b) * D + blockIdx.x * 64 + c] = red[0][b][c] + red[1][b][c] + red[2][b][c] + red[3][b][c];
}

__global__ void __launch_bounds__(256) k_ffn1(const u16* x, const float* part, const u16* wg, const u16* wu, float* hbuf, float* act) {
  __shared__ float xs[B * D];
  __shared__ float red[8];
  for (int i = threadIdx.x; i < B * D; i += 256) {
    float a = 0.f;
    for (int ks = 0; ks < 32; ++ks) a += part[ks * B * D + i];
    float h = rbf(bf2f(x[i]) + rbf(a));
    xs[i] = h;
    if (blockIdx.x == 0) hbuf[i] = h;
  }
  __syncthreads();
  ln_rows(xs, red);
  int n = blockIdx.x * 256 + threadIdx.x;
  float g[B] = {0, 0, 0, 0}, u[B] = {0, 0, 0, 0};
  for (int k = 0; k < D; ++k) {
    float a = bf2f(wg[(size_t)k * F + n]), c = bf2f(wu[(size_t)k * F + n]);
    for (int b = 0; b < B; ++b) { g[b] += xs[b * D + k] * a; u[b] += xs[b * D + k] * c; }
  }
  for (int b = 0; b < B; ++b) {
    float gg = rbf(g[b]);
    float sg = rbf(gg / (1.0f + __expf(-gg)));
    act[b * F + n] = rbf(sg * rbf(u[b]));
  }
}

__global__ void __launch_bounds__(256) k_ffn2(const float* act, const u16* wd, const float* hbuf, u16* out) {
  int n = blockIdx.x * 64 + (threadIdx.x & 63), kg = threadIdx.x >> 6;
  __shared__ float red[4][B][64];
  float acc[B] = {0, 0, 0, 0};
  for (int k = kg * (F / 4); k < (kg + 1) * (F / 4); ++k) {
    float w = bf2f(wd[(size_t)k * D + n]);
    for (int b = 0; b < B; ++b) acc[b] += act[b * F + k] * w;
  }
  for (int b = 0; b < B; ++b) red[kg][b][threadIdx.x & 63] = acc[b];
  __syncthreads();
  int b = kg, c = threadIdx.x & 63;
  float y = rbf(red[0][b][c] + red[1][b][c] + red[2][b][c] + red[3][b][c]);
  int col = blockIdx.x * 64 + c;
  out[b * D + col] = f2bf(hbuf[b * D + col] + y);
}

extern "C" void kernel_launch(void* const* d_in, const int* in_sizes, int n_in, void* d_out, int out_size, void* d_ws, size_t ws_size,
                              hipStream_t stream) {
  const u16* x = (const u16*)d_in[0];
  const u16* kc = (const u16*)d_in[1];
  const u16* vc = (const u16*)d_in[2];
  const u16* wq = (const u16*)d_in[3];
  const u16* wo = (const u16*)d_in[4];
  const u16* wg = (const u16*)d_in[6];
  const u16* wu = (const u16*)d_in[7];
  const u16* wd = (const u16*)d_in[8];
  float* ws = (float*)d_ws;
  float* q = ws;
  float* po = q + B * HQ * DH;
  float* pml = po + B * HQ * NSPLIT * DH;
  float* o = pml + B * HQ * NSPLIT * 2;
  float* part = o + B * HQ * DH;
  float* hbuf = part + 32 * B * D;
  float* act = hbuf + B * D;
  k_qproj<<<HQ * DH / 256, 256, 0, stream>>>(x, wq, q);
  k_attn<<<dim3(NSPLIT, HKV, B), 256, 0, stream>>>(q, kc, vc, po, pml);
  k_combine<<<B * HQ, 128, 0, stream>>>(po, pml, o);
  k_oproj<<<dim3(D / 64, 32), 256, 0, stream>>>(o, wo, part);
  k_ffn1<<<F / 256, 256, 0, stream>>>(x, part, wg, wu, hbuf, act);
  k_ffn2<<<D / 64, 256, 0, stream>>>(act, wd, hbuf, (u16*)d_out);
}
```

```cpp
#include <hip/hip_runtime.h>
#include <hip/hip_bf16.h>
#include <stdint.h>

typedef unsigned short u16;
typedef unsigned long long u64;
typedef unsigned v4u __attribute__((ext_vector_type(4)));
typedef unsigned v2u __attribute__((ext_vector_type(2)));
typedef float f32x4 __attribute__((ext_vector_type(4)));
typedef float f32x2 __attribute__((ext_vector_type(2)));
typedef __bf16 bf16x8 __attribute__((ext_vector_type(8)));
typedef __bf16 bf16x2 __attribute__((ext_vector_type(2)));
typedef short s16x4 __attribute__((ext_vector_type(4)));
#define LAS __attribute__((address_space(3)))
#define GAS __attribute__((address_space(1)))

#define WIN 3
#define TWIN 3
#define MAPB 0

constexpr int DM = 512, NQ = 4096, SK = 4096, DH = 128, FF = 1792;
constexpr int NF = 28;
constexpr int AUX_NT = 2, AUX_SC1 = 16;
constexpr unsigned SENT = 0xFFFFFFFFu;
constexpr int SPIN_CAP = 1 << 17;
constexpr float LOG2E = 1.4426950408889634f;

constexpr int WS_E = 0, WS_Q = 4096, WS_ML = WS_Q + 4 * 64 * 4 * 64 * 8, WS_P1 = WS_ML + 8192, WS_P2 = WS_P1 + 256 * 1024,
              WS_P3 = WS_P2 + 8 * 8192, WS_END = WS_P3 + NF * 8192;
constexpr int L_KS = 0, L_RED = 65536, L_QR = L_RED + 16384, L_QS = L_QR + 8192, L_MLW = L_QS + 1024, L_ST = L_MLW + 256, L_ST2 = L_ST + 256,
              L_H = L_ST2 + 256, L_ACT = L_H + 8192, L_END = L_ACT + 512;

#define STAMP(k) do {} while (0)
#define STAMP_OUT() do {} while (0)

__device__ __forceinline__ float u2f(unsigned u) { return __builtin_bit_cast(float, u); }
__device__ __forceinline__ unsigned f2u(float f) { return __builtin_bit_cast(unsigned, f); }
__device__ __forceinline__ float bf_lo(unsigned u) { return u2f(u << 16); }
__device__ __forceinline__ float bf_hi(unsigned u) { return u2f(u & 0xffff0000u); }
__device__ __forceinline__ unsigned pack_bf(float lo, float hi) {
  bf16x2 v; v.x = (__bf16)lo; v.y = (__bf16)hi; return __builtin_bit_cast(unsigned, v);
}
__device__ __forceinline__ float rbf(float f) { return (float)(__bf16)f; }
__device__ __forceinline__ unsigned nosent(unsigned u) { return u < 0xFFFFFFFEu ? u : 0xFFFFFFFEu; }
__device__ __forceinline__ bool has_sent(v4u v) { return max(max(v.x, v.y), max(v.z, v.w)) == SENT; }
template <int CTRL> __device__ __forceinline__ unsigned dppu(unsigned x) { return (unsigned)__builtin_amdgcn_mov_dpp((int)x, CTRL, 0xf, 0xf, true); }
template <int CTRL> __device__ __forceinline__ float dppf(float x) { return u2f(dppu<CTRL>(f2u(x))); }
constexpr int XOR1 = 0xB1, XOR2 = 0x4E, ROR8 = 0x128, BC0 = 0x00, BC1 = 0x55, BC2 = 0xAA, BC3 = 0xFF;
__device__ __forceinline__ f32x2 swap16(float x) { auto s = __builtin_amdgcn_permlane16_swap(f2u(x), f2u(x), false, false); return f32x2{u2f(s[0]), u2f(s[1])}; }
__device__ __forceinline__ f32x2 swap32(float x) { auto s = __builtin_amdgcn_permlane32_swap(f2u(x), f2u(x), false, false); return f32x2{u2f(s[0]), u2f(s[1])}; }
__device__ __forceinline__ float rows_max(float x) { f32x2 a = swap16(x); x = fmaxf(a.x, a.y); a = swap32(x); return fmaxf(a.x, a.y); }
__device__ __forceinline__ float rows_sum(float x) { f32x2 a = swap16(x); x = a.x + a.y; a = swap32(x); return a.x + a.y; }
__device__ __forceinline__ f32x4 mfma32(v4u a, v4u b, f32x4 c) {
  return __builtin_amdgcn_mfma_f32_16x16x32_bf16(__builtin_bit_cast(bf16x8, a), __builtin_bit_cast(bf16x8, b), c, 0, 0, 0);
}
__device__ __forceinline__ f32x4 mfma16(v2u a, v2u b, f32x4 c) {
  return __builtin_amdgcn_mfma_f32_16x16x16bf16_1k(__builtin_bit_cast(s16x4, a), __builtin_bit_cast(s16x4, b), c, 0, 0, 0);
}
__device__ __forceinline__ unsigned perm_lo(unsigned a, unsigned b) { return __builtin_amdgcn_perm(b, a, 0x05040100u); }
__device__ __forceinline__ unsigned perm_hi(unsigned a, unsigned b) { return __builtin_amdgcn_perm(b, a, 0x07060302u); }
__device__ __forceinline__ void transpose8(const v4u* in, v4u* o) {
#pragma unroll
  for (int k = 0; k < 4; ++k)
#pragma unroll
    for (int d2 = 0; d2 < 4; ++d2) {
      o[2 * d2][k] = perm_lo(in[2 * k][d2], in[2 * k + 1][d2]);
      o[2 * d2 + 1][k] = perm_hi(in[2 * k][d2], in[2 * k + 1][d2]);
    }
}
#define LDS_BARRIER() do { asm volatile("s_waitcnt lgkmcnt(0)" ::: "memory"); __builtin_amdgcn_s_barrier(); asm volatile("" ::: "memory"); } while (0)
#define CFENCE() asm volatile("" ::: "memory")
#define STR2(x) #x
#define STR(x) STR2(x)
#define PACE(n) do { __builtin_amdgcn_sched_barrier(0); asm volatile("s_waitcnt vmcnt(" STR(n) ")" ::: "memory"); } while (0)
#define PIN8(a) asm volatile("" : "+v"((a)[0]), "+v"((a)[1]), "+v"((a)[2]), "+v"((a)[3]), "+v"((a)[4]), "+v"((a)[5]), "+v"((a)[6]), "+v"((a)[7]))
#define MKRSRC(p, n) __builtin_amdgcn_make_buffer_rsrc((void*)(p), 0, (n), 0x00020000)
#define LD128(r, off, aux) __builtin_amdgcn_raw_buffer_load_b128((r), (off), 0, (aux))
#define LD64(r, off, aux) __builtin_amdgcn_raw_buffer_load_b64((r), (off), 0, (aux))
#define ST128(v, r, off) __builtin_amdgcn_raw_buffer_store_b128((v), (r), (off), 0, AUX_SC1)
#define ST64(v, r, off) __builtin_amdgcn_raw_buffer_store_b64((v), (r), (off), 0, AUX_SC1)
#define ST32(v, r, off) __builtin_amdgcn_raw_buffer_store_b32((v), (r), (off), 0, AUX_SC1)

template <int N> __device__ __forceinline__ void gather(__amdgpu_buffer_rsrc_t rs, v4u (&g)[N], const int (&off)[N]) {
#pragma unroll 1
  for (int spin = 0; spin < SPIN_CAP; ++spin) {
    bool bad[N], any = false;
#pragma unroll
    for (int n = 0; n < N; ++n) { bad[n] = has_sent(g[n]); any |= bad[n]; }
    if (!__any(any)) break;
    CFENCE();
#pragma unroll
    for (int n = 0; n < N; ++n)
      if (bad[n]) g[n] = LD128(rs, off[n], AUX_SC1);
  }
}
__device__ __forceinline__ f32x2 stats64(const float (&h)[4]) {
  float mu = ((h[0] + h[1]) + (h[2] + h[3])) * 0.25f, m2 = 0.f;
#pragma unroll
  for (int k = 0; k < 4; ++k) { const float d = h[k] - mu; m2 = fmaf(d, d, m2); }
  { const float o = dppf<XOR1>(mu), d = o - mu; m2 = fmaf(d * d, 2.0f, m2 + dppf<XOR1>(m2)); mu = (mu + o) * 0.5f; }
  { const float o = dppf<XOR2>(mu), d = o - mu; m2 = fmaf(d * d, 4.0f, m2 + dppf<XOR2>(m2)); mu = (mu + o) * 0.5f; }
  { const f32x2 a = swap16(mu), q = swap16(m2); const float d = a.x - a.y; m2 = fmaf(d * d, 8.0f, q.x + q.y); mu = (a.x + a.y) * 0.5f; }
  { const f32x2 a = swap32(mu), q = swap32(m2); const float d = a.x - a.y; m2 = fmaf(d * d, 16.0f, q.x + q.y); mu = (a.x + a.y) * 0.5f; }
  return f32x2{mu, m2};
}
__device__ __forceinline__ f32x2 stats_finish(LAS unsigned char* rec) {
  f32x4 st[4];
#pragma unroll
  for (int n = 0; n < 4; ++n) st[n] = *(LAS f32x4*)(rec + n * 16);
  const float mean = (((st[0].x + st[0].z) + (st[1].x + st[1].z)) + ((st[2].x + st[2].z) + (st[3].x + st[3].z))) * 0.125f;
  float q1 = 0.f, q2 = 0.f;
#pragma unroll
  for (int n = 0; n < 4; ++n) {
    const float d0 = st[n].x - mean, d1 = st[n].z - mean;
    q1 = fmaf(d0, d0, q1); q1 = fmaf(d1, d1, q1); q2 += st[n].y + st[n].w;
  }
  const float var = fmaf(q1, 64.0f, q2) * (1.0f / DM);
  return f32x2{rbf(mean), __builtin_amdgcn_rcpf(rbf(__builtin_amdgcn_sqrtf(rbf(rbf(var) + 1e-5f))))};
}
__device__ __forceinline__ int bid_attn(int g, int b, int s) { return MAPB ? (b * 8 + g) * 8 + s : (4 + b * 7 + s) * 8 + g; }
__device__ __forceinline__ int bid_tail(int t) { return MAPB ? t * 8 + 7 : (t & 3) * 8 + (t >> 2); }
__device__ __forceinline__ int q_slot(int bid) { return (((bid >> 3) * 2 + ((bid >> 2) & 1)) * 4 + (bid & 3)) * 512; }

__global__ void __launch_bounds__(512, 2)
fused_block(const u16* __restrict__ x, const u16* __restrict__ kc, const u16* __restrict__ vc, const u16* __restrict__ wq, const u16* __restrict__ wo,
            const u16* __restrict__ wg, const u16* __restrict__ wu, const u16* __restrict__ wd, u16* __restrict__ out, unsigned char* ws) {
  extern __shared__ __attribute__((aligned(16))) unsigned char smem[];
  LAS unsigned char* L = (LAS unsigned char*)smem;
  const int tid = threadIdx.x, lane = tid & 63, w = __builtin_amdgcn_readfirstlane(tid >> 6);
  const int bid = blockIdx.x;
  const bool tail = MAPB ? (bid & 7) == 7 : bid < 32;
  const int G = lane >> 4, c = lane & 15;
  const __amdgpu_buffer_rsrc_t rws = MKRSRC(ws, WS_END), rx = MKRSRC(x, 4 * DM * 2), rq = MKRSRC(wq, DM * NQ * 2);
  const v4u sent4 = {SENT, SENT, SENT, SENT};
  const f32x4 z4 = {0.f, 0.f, 0.f, 0.f};
  STAMP(0);
  const int sl = (bid >> 3) * 2 + ((bid >> 2) & 1), kq = bid & 3;

#define PROLOGUE_LOADS() \
  const v2u e2 = LD64(rws, WS_E, AUX_SC1); \
  const v2u XR = LD64(rx, (c >> 2) * 1024 + (w * 64 + G * 16 + (c & 3) * 4) * 2, 0); \
  const v2u XA = LD64(rx, (c >> 2) * 1024 + (kq * 128 + w * 16 + G * 4) * 2, 0); \
  v2u WQ[4]; \
  _Pragma("unroll") for (int j = 0; j < 4; ++j) WQ[j] = LD64(rq, (kq * 128 + w * 16 + G * 4 + j) * (NQ * 2) + sl * 128 + c * 8, AUX_NT);
#define QPROJ_STATS() { \
    const float xs[4] = {bf_lo(XR.x), bf_hi(XR.x), bf_lo(XR.y), bf_hi(XR.y)}; \
    const f32x2 s_ = stats64(xs); \
    if (G == 0 && (c & 3) == 0) *(LAS f32x2*)(L + L_ST + (c >> 2) * 64 + w * 8) = s_; \
    LDS_BARRIER(); }
#define QPROJ_MFMA() { \
    const f32x2 ms = stats_finish(L + L_ST + (c >> 2) * 64); \
    const v2u A = {pack_bf(rbf(bf_lo(XA.x) - ms.x) * ms.y, rbf(bf_hi(XA.x) - ms.x) * ms.y), pack_bf(rbf(bf_lo(XA.y) - ms.x) * ms.y, rbf(bf_hi(XA.y) - ms.x) * ms.y)}; \
    f32x4 o_; \
    _Pragma("unroll") for (int d2 = 0; d2 < 2; ++d2) { \
      o_[2 * d2] = mfma16(A, v2u{perm_lo(WQ[0][d2], WQ[1][d2]), perm_lo(WQ[2][d2], WQ[3][d2])}, z4).x; \
      o_[2 * d2 + 1] = mfma16(A, v2u{perm_hi(WQ[0][d2], WQ[1][d2]), perm_hi(WQ[2][d2], WQ[3][d2])}, z4).x; } \
    *(LAS f32x4*)(L + L_QR + w * 1024 + lane * 16) = o_; \
    LDS_BARRIER(); }
#define QPROJ_PUBLISH() \
  const u64 tag = (((u64)e2.y << 32 | e2.x) + 1ull) * 0x9E3779B97F4A7C15ull; \
  const unsigned tlo = (unsigned)(tag >> 24) & 0xFFu, thi = (unsigned)(tag >> 32); \
  if (w < 4) { \
    float a_[8]; \
    _Pragma("unroll") for (int n = 0; n < 8; ++n) a_[n] = *(LAS float*)(L + L_QR + n * 1024 + tid * 4); \
    const float t_ = ((a_[0] + a_[1]) + (a_[2] + a_[3])) + ((a_[4] + a_[5]) + (a_[6] + a_[7])); \
    ST64((v2u{(f2u(t_) & 0xFFFFFF00u) | tlo, thi}), rws, WS_Q + w * 131072 + q_slot(bid) + lane * 8); }

  if (!tail) {
    int g, b, s;
    if (MAPB) { s = bid & 7; g = (bid >> 3) & 7; b = bid >> 6; }
    else { g = bid & 7; const int k = (bid >> 3) - 4; b = k / 7; s = k - b * 7; }
    const int slot = (g * 4 + b) * 8 + s;
    const int rem = s < 2 ? 3 : 2, u0 = 146 * s + min(s, 2) + 18 * w + min(w, rem), nu = w < rem ? 19 : 18;
    const size_t kvo = ((size_t)(b * 8 + g) * SK + u0 * 4) * DH;
    const __amdgpu_buffer_rsrc_t rk = MKRSRC(kc + kvo, nu * 1024), rv = MKRSRC(vc + kvo, nu * 1024);
    if (w < 4) ST32(SENT, rws, WS_P1 + slot * 1024 + tid * 4);
    if (tid >= 256 && tid < 260) ST64((v2u{SENT, SENT}), rws, WS_ML + ((g * 4 + b) * 4 + (tid & 3)) * 64 + s * 8);
    CFENCE();
    PROLOGUE_LOADS()
    v4u K[19], V[19];
#define LOADK(u) K[u] = LD128(rk, (u) * 1024 + lane * 16, AUX_NT)
#define LOADV(u) do { PACE(WIN); V[u] = LD128(rv, (u) * 1024 + lane * 16, AUX_NT); __builtin_amdgcn_sched_barrier(0); } while (0)
    LOADK(0); LOADK(1); LOADK(2);
    __builtin_amdgcn_sched_barrier(0);
    QPROJ_STATS()
    LOADK(3);
    __builtin_amdgcn_sched_barrier(0);
    QPROJ_MFMA()
    LOADK(4);
    __builtin_amdgcn_sched_barrier(0);
    QPROJ_PUBLISH()
    STAMP(1);
#pragma unroll
    for (int u = 5; u < 19; ++u) { PACE(WIN); LOADK(u); }
    v4u qg[2];
    const int qo = WS_Q + b * 131072 + (g * 8 + w) * 2048 + lane * 16;
    PACE(WIN);
    qg[0] = LD128(rws, qo, AUX_SC1); qg[1] = LD128(rws, qo + 1024, AUX_SC1);
    LOADV(0); LOADV(1); LOADV(2);
#pragma unroll 1
    for (int spin = 0; spin < SPIN_CAP; ++spin) {
      const unsigned lo = ((qg[0].x ^ tlo) | (qg[0].z ^ tlo) | (qg[1].x ^ tlo) | (qg[1].z ^ tlo)) & 0xFFu;
      const unsigned hi = (qg[0].y ^ thi) | (qg[0].w ^ thi) | (qg[1].y ^ thi) | (qg[1].w ^ thi);
      if (!__any((lo | hi) != 0u)) break;
      CFENCE();
      qg[0] = LD128(rws, qo, AUX_SC1); qg[1] = LD128(rws, qo + 1024, AUX_SC1);
    }
    {
      const f32x2 a = swap32(u2f(qg[0].x & 0xFFFFFF00u) + u2f(qg[1].x & 0xFFFFFF00u));
      const f32x2 d = swap32(u2f(qg[0].z & 0xFFFFFF00u) + u2f(qg[1].z & 0xFFFFFF00u));
      if (lane < 32) *(LAS unsigned*)(L + L_QS + w * 128 + lane * 4) = pack_bf(a.x + a.y, d.x + d.y);
    }
    LDS_BARRIER();
    STAMP(2);
    LAS unsigned char* KS = L + L_KS + w * 8192;
    float p[19];
    v4u qf[4];
#pragma unroll
    for (int ks = 0; ks < 4; ++ks) qf[ks] = *(LAS v4u*)(L + L_QS + (c >> 2) * 256 + ks * 64 + G * 16);
#pragma unroll
    for (int t = 0; t < 5; ++t) {
      LOADV(3 + t);
#pragma unroll
      for (int r = 0; r < 4; ++r)
        if (4 * t + r < 19) *(LAS v4u*)(KS + (t & 1) * 4096 + (4 * G + r) * 256 + ((c ^ (4 * G + r)) * 16)) = K[4 * t + r];
      CFENCE();
      f32x4 d = z4;
#pragma unroll
      for (int ks = 0; ks < 4; ++ks) d = mfma32(*(LAS v4u*)(KS + (t & 1) * 4096 + c * 256 + (((4 * ks + G) ^ c) * 16)), qf[ks], d);
#pragma unroll
      for (int r = 0; r < 4; ++r)
        if (4 * t + r < 19) p[4 * t + r] = rbf(rbf(d[r]) * 0.08838834764831845f);
    }
    LOADV(8);
    if (nu < 19) p[18] = -INFINITY;
    float m = p[0];
#pragma unroll
    for (int k = 1; k < 19; ++k) m = fmaxf(m, p[k]);
    m = rows_max(m);
    float l = 0.f;
#pragma unroll
    for (int k = 0; k < 19; ++k) { p[k] = __builtin_amdgcn_exp2f((p[k] - m) * LOG2E); l += p[k]; }
    l = rows_sum(l);
    if (G == 0 && (c & 3) == 0) *(LAS f32x2*)(L + L_MLW + (c >> 2) * 64 + w * 8) = f32x2{m, l};
    LOADV(9);
    LDS_BARRIER();
    STAMP(3);
    v4u pf[2]; v2u pl;
    {
      f32x4 ml[4];
#pragma unroll
      for (int n = 0; n < 4; ++n) ml[n] = *(LAS f32x4*)(L + L_MLW + (c >> 2) * 64 + n * 16);
      float M = fmaxf(ml[0].x, ml[0].z);
#pragma unroll
      for (int n = 1; n < 4; ++n) M = fmaxf(M, fmaxf(ml[n].x, ml[n].z));
      float Ls = 0.f;
#pragma unroll
      for (int n = 0; n < 4; ++n) {
        Ls = fmaf(ml[n].y, __builtin_amdgcn_exp2f((ml[n].x - M) * LOG2E), Ls);
        Ls = fmaf(ml[n].w, __builtin_amdgcn_exp2f((ml[n].z - M) * LOG2E), Ls);
      }
      const float sc = __builtin_amdgcn_exp2f((m - M) * LOG2E) * __builtin_amdgcn_rcpf(Ls);
#pragma unroll
      for (int T = 0; T < 2; ++T)
#pragma unroll
        for (int k = 0; k < 4; ++k) pf[T][k] = pack_bf(p[T * 8 + 2 * k] * sc, p[T * 8 + 2 * k + 1] * sc);
      pl = v2u{pack_bf(p[16] * sc, p[17] * sc), pack_bf(p[18] * sc, 0.f)};
      if (w == 7 && G == 0 && (c & 3) == 0)
        ST64((v2u{nosent(f2u(M)), nosent(f2u(Ls))}), rws, WS_ML + ((g * 4 + b) * 4 + (c >> 2)) * 64 + s * 8);
    }
    LOADV(10); LOADV(11);
    f32x4 acc[8];
    {
      v4u bt[8];
      transpose8(V, bt);
#pragma unroll
      for (int d = 0; d < 8; ++d) acc[d] = mfma32(pf[0], bt[d], z4);
    }
    LOADV(12); LOADV(13); LOADV(14); LOADV(15); LOADV(16); LOADV(17); LOADV(18);
    {
      v4u bt[8];
      transpose8(V + 8, bt);
#pragma unroll
      for (int d = 0; d < 8; ++d) acc[d] = mfma32(pf[1], bt[d], acc[d]);
    }
    __builtin_amdgcn_sched_barrier(0);
#pragma unroll
    for (int d2 = 0; d2 < 4; ++d2) {
      acc[2 * d2] = mfma16(pl, v2u{perm_lo(V[16][d2], V[17][d2]), V[18][d2] & 0xFFFFu}, acc[2 * d2]);
      acc[2 * d2 + 1] = mfma16(pl, v2u{perm_hi(V[16][d2], V[17][d2]), V[18][d2] >> 16}, acc[2 * d2 + 1]);
    }
    *(LAS f32x4*)(L + L_RED + w * 2048 + lane * 32) = f32x4{acc[0].x, acc[1].x, acc[2].x, acc[3].x};
    *(LAS f32x4*)(L + L_RED + w * 2048 + lane * 32 + 16) = f32x4{acc[4].x, acc[5].x, acc[6].x, acc[7].x};
    LDS_BARRIER();
    STAMP(4);
    if (w < 4) {
      f32x2 a[8];
#pragma unroll
      for (int n = 0; n < 8; ++n) a[n] = *(LAS f32x2*)(L + L_RED + n * 2048 + tid * 8);
      const f32x2 t = ((a[0] + a[1]) + (a[2] + a[3])) + ((a[4] + a[5]) + (a[6] + a[7]));
      ST32(nosent(pack_bf(t.x, t.y)), rws, WS_P1 + slot * 1024 + tid * 4);
    }
    STAMP(5);
    STAMP_OUT();
    return;
  }

  const int t = MAPB ? bid >> 3 : (bid & 7) * 4 + (bid >> 3);
  const int g = t >> 2, j = t & 3;
  const bool isF = t < NF;
  ST32(SENT, rws, WS_P2 + t * 2048 + tid * 4);
  if (isF) ST128(sent4, rws, WS_P3 + t * 8192 + tid * 16);
  CFENCE();
  PROLOGUE_LOADS()
  v4u WO[16], GU[16], WD[8];
  {
    v4u R[8];
    const __amdgpu_buffer_rsrc_t ro = MKRSRC(wo, NQ * DM * 2);
    const int oo = (g * 512 + w * 64 + G * 8) * (DM * 2) + j * 256 + c * 16;
#define LOADO(n) R[(n) & 7] = LD128(ro, oo + (((n) >> 3) * 32 + ((n) & 7)) * (DM * 2), AUX_NT)
    LOADO(0); LOADO(1); LOADO(2);
    __builtin_amdgcn_sched_barrier(0);
    QPROJ_STATS()
    LOADO(3);
    __builtin_amdgcn_sched_barrier(0);
    QPROJ_MFMA()
    LOADO(4);
    __builtin_amdgcn_sched_barrier(0);
    QPROJ_PUBLISH()
    STAMP(1);
    v4u R2[8], R3[8], R4[8], R5[8];
#pragma unroll
    for (int n = 5; n < 8; ++n) { PACE(TWIN); LOADO(n); }
#pragma unroll
    for (int n = 0; n < 8; ++n) { PACE(TWIN); R2[n] = LD128(ro, oo + (32 + n) * (DM * 2), AUX_NT); }
    transpose8(R, WO); PIN8(WO);
    const int ft = isF ? t : 0;
    const GAS v4u* gp = (const GAS v4u*)((c < 8 ? wg : wu) + (size_t)(w * 64 + G * 16) * FF + ft * 64 + (c & 7) * 8);
#pragma unroll
    for (int n = 0; n < 8; ++n) { PACE(TWIN); R3[n] = __builtin_nontemporal_load(gp + n * (FF / 8)); }
    transpose8(R2, WO + 8); PIN8(WO + 8);
#pragma unroll
    for (int n = 0; n < 8; ++n) { PACE(TWIN); R4[n] = __builtin_nontemporal_load(gp + (8 + n) * (FF / 8)); }
    transpose8(R3, GU); PIN8(GU);
    const int who = lane < 32 ? bid_attn(g, min(lane, 27) / 7, min(lane, 27) % 7) : bid_tail(lane - 32);
    const int fo = WS_Q + q_slot(who);
    PACE(TWIN);
    v2u fl = LD64(rws, fo, AUX_SC1);
    const __amdgpu_buffer_rsrc_t rd = MKRSRC(wd, FF * DM * 2);
#pragma unroll
    for (int n = 0; n < 8; ++n) { PACE(TWIN); R5[n] = LD128(rd, (ft * 64 + (c >> 3) * 32 + G * 8 + n) * (DM * 2) + w * 128 + (c & 7) * 16, AUX_NT); }
    transpose8(R4, GU + 8); PIN8(GU + 8);
#pragma unroll 1
    for (int spin = 0; spin < SPIN_CAP; ++spin) {
      if (!__any((((fl.x ^ tlo) & 0xFFu) | (fl.y ^ thi)) != 0u)) break;
      CFENCE();
      fl = LD64(rws, fo, AUX_SC1);
    }
    transpose8(R5, WD); PIN8(WD);
    CFENCE();
  }
  STAMP(2);
  v4u hg[8];
  int ho[8];
  {
    const int rb = c >> 2, rs = c & 3;
    v4u mlg[1], pg[4];
    const int mo[1] = {WS_ML + ((g * 4 + rb) * 4 + (w >> 1)) * 64 + rs * 16};
    int po[4];
#pragma unroll
    for (int n = 0; n < 4; ++n) {
      po[n] = WS_P1 + ((g * 4 + rb) * 8 + 2 * rs + (n >> 1)) * 1024 + w * 128 + (n & 1) * 64 + G * 16;
      pg[n] = (rs == 3 && n >= 2) ? v4u{0u, 0u, 0u, 0u} : sent4;
    }
    float c0, c1;
    {
      v4u mv = sent4;
#pragma unroll 1
      for (int spin = 0; spin < SPIN_CAP; ++spin) {
        mv = LD128(rws, mo[0], AUX_SC1);
        if (rs == 3) { mv.z = f2u(-INFINITY); mv.w = 0u; }
        if (!__any(has_sent(mv))) break;
        CFENCE();
      }
      float M = fmaxf(u2f(mv.x), u2f(mv.z));
      M = fmaxf(M, dppf<XOR1>(M)); M = fmaxf(M, dppf<XOR2>(M));
      c0 = u2f(mv.y) * __builtin_amdgcn_exp2f((u2f(mv.x) - M) * LOG2E);
      c1 = u2f(mv.w) * __builtin_amdgcn_exp2f((u2f(mv.z) - M) * LOG2E);
      float Ls = c0 + c1;
      Ls += dppf<XOR1>(Ls); Ls += dppf<XOR2>(Ls);
      const float r = __builtin_amdgcn_rcpf(Ls);
      c0 *= r; c1 *= r;
      asm volatile("" : "+v"(c0), "+v"(c1));
    }
    STAMP(3);
    gather<4>(rws, pg, po);
    STAMP(6);
#pragma unroll
    for (int n = 0; n < 8; ++n) { ho[n] = WS_P2 + n * 8192 + w * 1024 + lane * 16; hg[n] = LD128(rws, ho[n], AUX_SC1); }
    __builtin_amdgcn_sched_barrier(0);
    f32x4 acc[8];
#pragma unroll
    for (int ks = 0; ks < 2; ++ks) {
      v4u A;
#pragma unroll
      for (int k = 0; k < 4; ++k)
        A[k] = pack_bf(fmaf(c1, bf_lo(pg[2 + ks][k]), c0 * bf_lo(pg[ks][k])), fmaf(c1, bf_hi(pg[2 + ks][k]), c0 * bf_hi(pg[ks][k])));
#pragma unroll
      for (int d = 0; d < 8; ++d) acc[d] = mfma32(A, WO[ks * 8 + d], ks ? acc[d] : z4);
    }
    f32x4 o0, o1;
#pragma unroll
    for (int d = 0; d < 4; ++d) {
      o0[d] = (acc[d].x + acc[d].y) + (acc[d].z + acc[d].w);
      o1[d] = (acc[4 + d].x + acc[4 + d].y) + (acc[4 + d].z + acc[4 + d].w);
    }
    *(LAS f32x4*)(L + L_RED + w * 2048 + (c >> 1) * 256 + G * 64 + (c & 1) * 32) = o0;
    *(LAS f32x4*)(L + L_RED + w * 2048 + (c >> 1) * 256 + G * 64 + (c & 1) * 32 + 16) = o1;
    LDS_BARRIER();
    float a[8];
#pragma unroll
    for (int n = 0; n < 8; ++n) a[n] = *(LAS float*)(L + L_RED + n * 2048 + tid * 4);
    ST32(nosent(f2u(((a[0] + a[1]) + (a[2] + a[3])) + ((a[4] + a[5]) + (a[6] + a[7])))), rws, WS_P2 + t * 2048 + tid * 4);
    STAMP(7);
  }
  gather<8>(rws, hg, ho);
  STAMP(8);
  float h[4];
  {
    const float xs[4] = {bf_lo(XR.x), bf_hi(XR.x), bf_lo(XR.y), bf_hi(XR.y)};
#pragma unroll
    for (int k = 0; k < 4; ++k) {
      const float a = ((u2f(hg[0][k]) + u2f(hg[1][k])) + (u2f(hg[2][k]) + u2f(hg[3][k]))) + ((u2f(hg[4][k]) + u2f(hg[5][k])) + (u2f(hg[6][k]) + u2f(hg[7][k])));
      h[k] = rbf(xs[k] + rbf(a));
    }
    const f32x2 s_ = stats64(h);
    if (G == 0 && (c & 3) == 0) *(LAS f32x2*)(L + L_ST2 + (c >> 2) * 64 + w * 8) = s_;
    *(LAS f32x4*)(L + L_H + (c >> 2) * 2048 + (w * 64 + G * 16 + (c & 3) * 4) * 4) = f32x4{h[0], h[1], h[2], h[3]};
  }
  LDS_BARRIER();
  if (isF) {
    const f32x2 ms = stats_finish(L + L_ST2 + (c >> 2) * 64);
    const unsigned e0 = pack_bf(rbf(h[0] - ms.x) * ms.y, rbf(h[1] - ms.x) * ms.y), e1 = pack_bf(rbf(h[2] - ms.x) * ms.y, rbf(h[3] - ms.x) * ms.y);
    const v4u A0 = {dppu<BC0>(e0), dppu<BC0>(e1), dppu<BC1>(e0), dppu<BC1>(e1)};
    const v4u A1 = {dppu<BC2>(e0), dppu<BC2>(e1), dppu<BC3>(e0), dppu<BC3>(e1)};
    f32x4 acc[8];
#pragma unroll
    for (int d = 0; d < 8; ++d) acc[d] = mfma32(A0, GU[d], z4);
#pragma unroll
    for (int d = 0; d < 8; ++d) acc[d] = mfma32(A1, GU[8 + d], acc[d]);
    LAS unsigned char* gr = L + L_KS + w * 2048 + G * 512 + ((c >> 2) & 1) * 256 + (c >> 3) * 128 + (c & 3) * 32;
    *(LAS f32x4*)(gr) = f32x4{acc[0].x, acc[1].x, acc[2].x, acc[3].x};
    *(LAS f32x4*)(gr + 16) = f32x4{acc[4].x, acc[5].x, acc[6].x, acc[7].x};
    LDS_BARRIER();
    STAMP(9);
    {
      float a[8];
#pragma unroll
      for (int n = 0; n < 8; ++n) a[n] = *(LAS float*)(L + L_KS + n * 2048 + tid * 4);
      const f32x2 gu = swap32(rbf(((a[0] + a[1]) + (a[2] + a[3])) + ((a[4] + a[5]) + (a[6] + a[7]))));
      const float sg = rbf(gu.x * __builtin_amdgcn_rcpf(1.0f + __builtin_amdgcn_exp2f(-gu.x * LOG2E)));
      if (lane < 32) *(LAS u16*)(L + L_ACT + w * 64 + lane * 2) = (u16)pack_bf(sg * gu.y, 0.f);
    }
    LDS_BARRIER();
    const v4u A = *(LAS v4u*)(L + L_ACT + (c >> 2) * 128 + (c & 1) * 64 + G * 16);
#pragma unroll
    for (int d = 0; d < 8; ++d) acc[d] = mfma32(A, WD[d], z4);
    v4u o;
#pragma unroll
    for (int k = 0; k < 4; ++k) {
      const float send = c < 8 ? acc[k + 4].x : acc[k].y, keep = c < 8 ? acc[k].x : acc[k + 4].y;
      o[k] = nosent(f2u(keep + dppf<ROR8>(send)));
    }
    ST128(o, rws, WS_P3 + t * 8192 + tid * 16);
    STAMP(10);
  }
  if (w < 2) {
    const int r = t * 2 + w, col = (r >> 3) * 64 + (lane & 7) * 8 + (r & 1) * 4, rb = (r >> 1) & 3;
    v4u pg[4];
    int po[4];
#pragma unroll
    for (int n = 0; n < 4; ++n) {
      po[n] = WS_P3 + ((lane >> 3) + 8 * n) * 8192 + r * 128 + (lane & 7) * 16;
      pg[n] = (n == 3 && lane >= 32) ? v4u{0u, 0u, 0u, 0u} : sent4;
    }
    const f32x4 hv = *(LAS f32x4*)(L + L_H + rb * 2048 + col * 4);
    gather<4>(rws, pg, po);
    STAMP(11);
    float y[4];
#pragma unroll
    for (int q = 0; q < 4; ++q) {
      float a = (u2f(pg[0][q]) + u2f(pg[1][q])) + (u2f(pg[2][q]) + u2f(pg[3][q]));
      a += dppf<ROR8>(a);
      y[q] = hv[q] + rbf(rows_sum(a));
    }
    if (lane < 8) *(v2u*)(out + rb * DM + col) = v2u{pack_bf(y[0], y[1]), pack_bf(y[2], y[3])};
    if (r == 0 && lane == 0) {
      const u64 e = ((u64)e2.y << 32 | e2.x) + 1ull;
      ST64((v2u{(unsigned)e, (unsigned)(e >> 32)}), rws, WS_E);
    }
    STAMP(12);
  }
  STAMP_OUT();
}

extern "C" void kernel_launch(void* const* d_in, const int* in_sizes, int n_in, void* d_out, int out_size, void* d_ws, size_t ws_size,
                              hipStream_t stream) {
  (void)hipFuncSetAttribute((const void*)fused_block, hipFuncAttributeMaxDynamicSharedMemorySize, L_END);
  fused_block<<<256, 512, L_END, stream>>>((const u16*)d_in[0], (const u16*)d_in[1], (const u16*)d_in[2], (const u16*)d_in[3], (const u16*)d_in[4],
                                           (const u16*)d_in[6], (const u16*)d_in[7], (const u16*)d_in[8], (u16*)d_out, (unsigned char*)d_ws);
}
```

```cpp
#include <hip/hip_runtime.h>
#include <hip/hip_bf16.h>
#include <stdint.h>

typedef unsigned short u16;
typedef unsigned v4u __attribute__((ext_vector_type(4)));

constexpr int B = 4, D = 512, HQ = 32, HKV = 8, SK = 4096, DH = 128, F = 1792;
constexpr int NSPLIT = 8, KPS = SK / NSPLIT;
#define MIMIC 1

__device__ __forceinline__ float bf2f(u16 v) { return __builtin_bit_cast(float, (unsigned)v << 16); }
__device__ __forceinline__ u16 f2bf(float f) { __bf16 b = (__bf16)f; return __builtin_bit_cast(u16, b); }
__device__ __forceinline__ float rbf(float f) {
  return bf2f(f2bf(f));
}

__device__ float block_sum(float v, float* red) {
  for (int o = 32; o; o >>= 1) v += __shfl_xor(v, o);
  int w = threadIdx.x >> 6;
  __syncthreads();
  if ((threadIdx.x & 63) == 0) red[w] = v;
  __syncthreads();
  float s = 0.f;
  for (int i = 0; i < (int)(blockDim.x >> 6); ++i) s += red[i];
  return s;
}

__device__ void ln_rows(float* xs, float* red) {
  for (int b = 0; b < B; ++b) {
    float s = 0.f;
    for (int i = threadIdx.x; i < D; i += blockDim.x) s += xs[b * D + i];
    float mean = block_sum(s, red) * (1.0f / D);
    float s2 = 0.f;
    for (int i = threadIdx.x; i < D; i += blockDim.x) { float d = xs[b * D + i] - mean; s2 += d * d; }
    float var = block_sum(s2, red) * (1.0f / D);
    float m = rbf(mean), v = rbf(var);
    float sd = rbf(sqrtf(rbf(v + 1e-5f)));
    __syncthreads();
    for (int i = threadIdx.x; i < D; i += blockDim.x) xs[b * D + i] = rbf(rbf(xs[b * D + i] - m) / sd);
    __syncthreads();
  }
}

__global__ void __launch_bounds__(256) k_qproj(const u16* x, const u16* wq, float* q, unsigned long long* tw) {
  if (blockIdx.x == 0 && threadIdx.x == 0) *tw = __builtin_amdgcn_s_memrealtime();
  __shared__ float xs[B * D];
  __shared__ float red[8];
  for (int i = threadIdx.x; i < B * D; i += 256) xs[i] = bf2f(x[i]);
  __syncthreads();
  ln_rows(xs, red);
  int n = blockIdx.x * 256 + threadIdx.x;
  float acc[B] = {0, 0, 0, 0};
  for (int k = 0; k < D; ++k) {
    float w = bf2f(wq[(size_t)k * (HQ * DH) + n]);
    for (int b = 0; b < B; ++b) acc[b] += xs[b * D + k] * w;
  }
  for (int b = 0; b < B; ++b) q[b * HQ * DH + n] = rbf(acc[b]);
}

__global__ void __launch_bounds__(256) k_attn(const float* q, const u16* kc, const u16* vc, float* po, float* pml) {
  __shared__ float qs[4 * DH];
  __shared__ float ps[4 * KPS];
  __shared__ float red[8];
  int s = blockIdx.x, kvh = blockIdx.y, b = blockIdx.z;
  for (int i = threadIdx.x; i < 4 * DH; i += 256) qs[i] = q[b * HQ * DH + kvh * 4 * DH + i];
  __syncthreads();
  const u16* kb = kc + ((size_t)(b * HKV + kvh) * SK + s * KPS) * DH;
  const u16* vb = vc + ((size_t)(b * HKV + kvh) * SK + s * KPS) * DH;
  for (int key = threadIdx.x; key < KPS; key += 256) {
    float a[4] = {0, 0, 0, 0};
    for (int d = 0; d < DH; d += 8) {
      v4u kk = *(const v4u*)(kb + (size_t)key * DH + d);
      for (int j = 0; j < 4; ++j) {
        float lo = __builtin_bit_cast(float, kk[j] << 16), hi = __builtin_bit_cast(float, kk[j] & 0xffff0000u);
        for (int h = 0; h < 4; ++h) a[h] += lo * qs[h * DH + d + 2 * j] + hi * qs[h * DH + d + 2 * j + 1];
      }
    }
    for (int h = 0; h < 4; ++h) ps[h * KPS + key] = rbf(rbf(a[h]) * 0.08838834764831845f);
  }
  __syncthreads();
  for (int h = 0; h < 4; ++h) {
    float m = -INFINITY;
    for (int i = threadIdx.x; i < KPS; i += 256) m = fmaxf(m, ps[h * KPS + i]);
    for (int o = 32; o; o >>= 1) m = fmaxf(m, __shfl_xor(m, o));
    __syncthreads();
    if ((threadIdx.x & 63) == 0) red[threadIdx.x >> 6] = m;
    __syncthreads();
    m = fmaxf(fmaxf(red[0], red[1]), fmaxf(red[2], red[3]));
    float l = 0.f;
    for (int i = threadIdx.x; i < KPS; i += 256) { float p = __expf(ps[h * KPS + i] - m); ps[h * KPS + i] = p; l += p; }
    l = block_sum(l, red);
    if (threadIdx.x == 0) { size_t r = ((size_t)(b * HQ + kvh * 4 + h) * NSPLIT + s); pml[2 * r] = m; pml[2 * r + 1] = l; }
  }
  __syncthreads();
  int d = threadIdx.x & 127, hp = threadIdx.x >> 7;
  float o0 = 0.f, o1 = 0.f;
  for (int key = 0; key < KPS; ++key) {
    float v = bf2f(vb[(size_t)key * DH + d]);
    o0 += ps[(2 * hp) * KPS + key] * v;
    o1 += ps[(2 * hp + 1) * KPS + key] * v;
  }
  po[((size_t)(b * HQ + kvh * 4 + 2 * hp) * NSPLIT + s) * DH + d] = o0;
  po[((size_t)(b * HQ + kvh * 4 + 2 * hp + 1) * NSPLIT + s) * DH + d] = o1;
}

__global__ void __launch_bounds__(128) k_combine(const float* po, const float* pml, float* o) {
  int r = blockIdx.x, d = threadIdx.x;
  float M = -INFINITY;
  for (int s = 0; s < NSPLIT; ++s) M = fmaxf(M, pml[2 * (r * NSPLIT + s)]);
  float L = 0.f, acc = 0.f;
  for (int s = 0; s < NSPLIT; ++s) {
    float w = __expf(pml[2 * (r * NSPLIT + s)] - M);
    L += w * pml[2 * (r * NSPLIT + s) + 1];
    acc += w * po[(size_t)(r * NSPLIT + s) * DH + d];
  }
  o[r * DH + d] = rbf(acc / L);
}

__global__ void __launch_bounds__(256) k_oproj(const float* o, const u16* wo, float* part) {
  int n = blockIdx.x * 64 + (threadIdx.x & 63), kg = threadIdx.x >> 6, ks = blockIdx.y;
  __shared__ float red[4][B][64];
  float acc[B] = {0, 0, 0, 0};
  for (int k = ks * 128 + kg * 32; k < ks * 128 + kg * 32 + 32; ++k) {
    float w = bf2f(wo[(size_t)k * D + n]);
    for (int b = 0; b < B; ++b) acc[b] += o[b * HQ * DH + k] * w;
  }
  for (int b = 0; b < B; ++b) red[kg][b][threadIdx.x & 63] = acc[b];
  __syncthreads();
  int b = kg, c = threadIdx.x & 63;
  part[(ks * B + b) * D + blockIdx.x * 64 + c] = red[0][b][c] + red[1][b][c] + red[2][b][c] + red[3][b][c];
}

__global__ void __launch_bounds__(256) k_ffn1(const u16* x, const float* part, const u16* wg, const u16* wu, float* hbuf, float* act) {
  __shared__ float xs[B * D];
  __shared__ float red[8];
  for (int i = threadIdx.x; i < B * D; i += 256) {
    float a = 0.f;
    for (int ks = 0; ks < 32; ++ks) a += part[ks * B * D + i];
    float h = rbf(bf2f(x[i]) + rbf(a));
    xs[i] = h;
    if (blockIdx.x == 0) hbuf[i] = h;
  }
  __syncthreads();
  ln_rows(xs, red);
  int n = blockIdx.x * 256 + threadIdx.x;
  float g[B] = {0, 0, 0, 0}, u[B] = {0, 0, 0, 0};
  for (int k = 0; k < D; ++k) {
    float a = bf2f(wg[(size_t)k * F + n]), c = bf2f(wu[(size_t)k * F + n]);
    for (int b = 0; b < B; ++b) { g[b] += xs[b * D + k] * a; u[b] += xs[b * D + k] * c; }
  }
  for (int b = 0; b < B; ++b) {
    float gg = rbf(g[b]);
    float sg = rbf(gg / (1.0f + __expf(-gg)));
    act[b * F + n] = rbf(sg * rbf(u[b]));
  }
}

__global__ void __launch_bounds__(256) k_ffn2(const float* act, const u16* wd, const float* hbuf, u16* out, const unsigned long long* tw) {
  int n = blockIdx.x * 64 + (threadIdx.x & 63), kg = threadIdx.x >> 6;
  __shared__ float red[4][B][64];
  float acc[B] = {0, 0, 0, 0};
  for (int k = kg * (F / 4); k < (kg + 1) * (F / 4); ++k) {
    float w = bf2f(wd[(size_t)k * D + n]);
    for (int b = 0; b < B; ++b) acc[b] += act[b * F + k] * w;
  }
  for (int b = 0; b < B; ++b) red[kg][b][threadIdx.x & 63] = acc[b];
  __syncthreads();
  int b = kg, c = threadIdx.x & 63;
  float y = rbf(red[0][b][c] + red[1][b][c] + red[2][b][c] + red[3][b][c]);
  int col = blockIdx.x * 64 + c;
  const unsigned long long t0 = *tw;
  while (__builtin_amdgcn_s_memrealtime() - t0 < 40000) __builtin_amdgcn_s_sleep(1);
  out[b * D + col] = f2bf(hbuf[b * D + col] + y);
}

extern "C" void kernel_launch(void* const* d_in, const int* in_sizes, int n_in, void* d_out, int out_size, void* d_ws, size_t ws_size,
                              hipStream_t stream) {
  const u16* x = (const u16*)d_in[0];
  const u16* kc = (const u16*)d_in[1];
  const u16* vc = (const u16*)d_in[2];
  const u16* wq = (const u16*)d_in[3];
  const u16* wo = (const u16*)d_in[4];
  const u16* wg = (const u16*)d_in[6];
  const u16* wu = (const u16*)d_in[7];
  const u16* wd = (const u16*)d_in[8];
  float* ws = (float*)d_ws;
  float* q = ws;
  float* po = q + B * HQ * DH;
  float* pml = po + B * HQ * NSPLIT * DH;
  float* o = pml + B * HQ * NSPLIT * 2;
  float* part = o + B * HQ * DH;
  float* hbuf = part + 32 * B * D;
  float* act = hbuf + B * D;
  k_qproj<<<HQ * DH / 256, 256, 0, stream>>>(x, wq, q, (unsigned long long*)((char*)d_ws + (64u << 20)));
  k_attn<<<dim3(NSPLIT, HKV, B), 256, 0, stream>>>(q, kc, vc, po, pml);
  k_combine<<<B * HQ, 128, 0, stream>>>(po, pml, o);
  k_oproj<<<dim3(D / 64, 32), 256, 0, stream>>>(o, wo, part);
  k_ffn1<<<F / 256, 256, 0, stream>>>(x, part, wg, wu, hbuf, act);
  k_ffn2<<<D / 64, 256, 0, stream>>>(act, wd, hbuf, (u16*)d_out, (const unsigned long long*)((char*)d_ws + (64u << 20)));
}
```

```cpp
#include <hip/hip_runtime.h>
#include <hip/hip_bf16.h>
#include <stdint.h>

typedef unsigned short u16;
typedef unsigned long long u64;
typedef unsigned v4u __attribute__((ext_vector_type(4)));
typedef unsigned v2u __attribute__((ext_vector_type(2)));
typedef unsigned v16u __attribute__((ext_vector_type(16)));
typedef float f32x4 __attribute__((ext_vector_type(4)));
typedef float f32x2 __attribute__((ext_vector_type(2)));
typedef __bf16 bf16x8 __attribute__((ext_vector_type(8)));
typedef __bf16 bf16x2 __attribute__((ext_vector_type(2)));
typedef short s16x4 __attribute__((ext_vector_type(4)));
#define LAS __attribute__((address_space(3)))
#define GAS __attribute__((address_space(1)))

constexpr int DM = 512, NQ = 4096, SK = 4096, DH = 128, FF = 1792;
constexpr int NF = 28;
constexpr int AUX_NT = 2, AUX_SC1 = 16;
constexpr unsigned EPOCH_STEP = 0x01010101u;
constexpr int SPIN_CAP = 1 << 17;
constexpr float LOG2E = 1.4426950408889634f;
constexpr unsigned TAG_MUL = 0x9E3779B1u;
constexpr int RS = 4;
constexpr int KBAR = 12;
constexpr int QPOS = 30;
constexpr int VSPLIT = 38;
constexpr int ODELAY = 800, FDELAY = 950, SLOWADJ = 200, MDELAY = 1100;

constexpr int DUP = 8192;
constexpr int WS_E = 0, WS_NEAR = 256, WS_Q = 4096, WS_ML = WS_Q + 32 * 2048, WS_PROBE = 81920, WS_P1 = WS_PROBE + 16384, WS_P2 = WS_P1 + 192 * 4096,
              WS_H = WS_P2 + 32 * 16384, WS_P3 = WS_H + 16384, WS_END = WS_P3 + NF * 16384;
static_assert(WS_ML + 192 * 64 <= WS_PROBE && WS_PROBE % 16384 == 0 && WS_END <= (2 << 20), "workspace map");
__device__ __forceinline__ int p1_off(int slot) { return WS_P1 + (slot >> 2) * 16384 + (slot & 3) * 2048; }
constexpr int KW = 36 * 1024, NUP = 200;
constexpr int L_KS = 0, L_RED = 0, L_P = 4 * KW, L_QS = L_P + 16 * NUP * 2, L_MLW = L_QS + 1024, L_FLAG = L_MLW + 128, L_END = L_FLAG + 32;
constexpr int L_QR = 0, L_GR = 16384, L_CS = 40960, L_ST = 45056, L_ST2 = L_ST + 256, L_ACT = L_ST2 + 256, L_ML = L_ACT + 512,
              L_NEAR = L_ML + 2048, L_O = L_NEAR + 16;
constexpr int CODE_BYTES = 19840;

__device__ __forceinline__ float u2f(unsigned u) { return __builtin_bit_cast(float, u); }
__device__ __forceinline__ unsigned f2u(float f) { return __builtin_bit_cast(unsigned, f); }
__device__ __forceinline__ float bf_lo(unsigned u) { return u2f(u << 16); }
__device__ __forceinline__ float bf_hi(unsigned u) { return u2f(u & 0xffff0000u); }
__device__ __forceinline__ unsigned pack_bf(float lo, float hi) {
  bf16x2 v; v.x = (__bf16)lo; v.y = (__bf16)hi; return __builtin_bit_cast(unsigned, v);
}
__device__ __forceinline__ float rbf(float f) { return (float)(__bf16)f; }
__device__ __forceinline__ unsigned mark(float v, unsigned t8) { return __builtin_amdgcn_perm(f2u(v), t8, 0x07060500u); }
template <int K> __device__ __forceinline__ unsigned mark_k(float v, unsigned e) { return __builtin_amdgcn_perm(f2u(v), e, 0x07060500u + K); }
template <int CTRL> __device__ __forceinline__ unsigned dppu(unsigned x) { return (unsigned)__builtin_amdgcn_mov_dpp((int)x, CTRL, 0xf, 0xf, true); }
template <int CTRL> __device__ __forceinline__ float dppf(float x) { return u2f(dppu<CTRL>(f2u(x))); }
constexpr int XOR1 = 0xB1, XOR2 = 0x4E, HMIR = 0x141, ROR4 = 0x124, ROR8 = 0x128, BC0 = 0x00, BC1 = 0x55, BC2 = 0xAA, BC3 = 0xFF;
__device__ __forceinline__ f32x2 swap16(float x) { auto s = __builtin_amdgcn_permlane16_swap(f2u(x), f2u(x), false, false); return f32x2{u2f(s[0]), u2f(s[1])}; }
__device__ __forceinline__ f32x2 swap32(float x) { auto s = __builtin_amdgcn_permlane32_swap(f2u(x), f2u(x), false, false); return f32x2{u2f(s[0]), u2f(s[1])}; }
__device__ __forceinline__ float rows_max(float x) { f32x2 a = swap16(x); x = fmaxf(a.x, a.y); a = swap32(x); return fmaxf(a.x, a.y); }
__device__ __forceinline__ float rows_sum(float x) { f32x2 a = swap16(x); x = a.x + a.y; a = swap32(x); return a.x + a.y; }
__device__ __forceinline__ f32x4 mfma32(v4u a, v4u b, f32x4 c) {
  return __builtin_amdgcn_mfma_f32_16x16x32_bf16(__builtin_bit_cast(bf16x8, a), __builtin_bit_cast(bf16x8, b), c, 0, 0, 0);
}
__device__ __forceinline__ f32x4 mfma16(v2u a, v2u b, f32x4 c) {
  return __builtin_amdgcn_mfma_f32_16x16x16bf16_1k(__builtin_bit_cast(s16x4, a), __builtin_bit_cast(s16x4, b), c, 0, 0, 0);
}
__device__ __forceinline__ float dot2(unsigned a, unsigned b, float c) {
  return __builtin_amdgcn_fdot2_f32_bf16(__builtin_bit_cast(bf16x2, a), __builtin_bit_cast(bf16x2, b), c, false);
}
__device__ __forceinline__ unsigned perm_lo(unsigned a, unsigned b) { return __builtin_amdgcn_perm(b, a, 0x05040100u); }
__device__ __forceinline__ unsigned perm_hi(unsigned a, unsigned b) { return __builtin_amdgcn_perm(b, a, 0x07060302u); }
__device__ __forceinline__ void transpose8(const v4u* in, v4u* o) {
#pragma unroll
  for (int k = 0; k < 4; ++k)
#pragma unroll
    for (int d2 = 0; d2 < 4; ++d2) {
      o[2 * d2][k] = perm_lo(in[2 * k][d2], in[2 * k + 1][d2]);
      o[2 * d2 + 1][k] = perm_hi(in[2 * k][d2], in[2 * k + 1][d2]);
    }
}
#define LDS_BARRIER() do { asm volatile("s_waitcnt lgkmcnt(0)" ::: "memory"); __builtin_amdgcn_s_barrier(); asm volatile("" ::: "memory"); } while (0)
#define CFENCE() asm volatile("" ::: "memory")
#define SB() __builtin_amdgcn_sched_barrier(0)
#define PACE() do { SB(); asm volatile("s_waitcnt vmcnt(5)" ::: "memory"); } while (0)
#define PIN8(a) asm volatile("" : "+v"((a)[0]), "+v"((a)[1]), "+v"((a)[2]), "+v"((a)[3]), "+v"((a)[4]), "+v"((a)[5]), "+v"((a)[6]), "+v"((a)[7]))
#define PIN4(a) asm volatile("" : "+v"((a)[0]), "+v"((a)[1]), "+v"((a)[2]), "+v"((a)[3]))
#define SUM8(a) ((((a)[0] + (a)[2]) + ((a)[4] + (a)[6])) + (((a)[1] + (a)[3]) + ((a)[5] + (a)[7])))
#define MKRSRC(p, n) __builtin_amdgcn_make_buffer_rsrc((void*)(p), 0, (n), 0x00020000)
#define LD128(r, off, aux) __builtin_amdgcn_raw_buffer_load_b128((r), (off), 0, (aux))
#define LD64(r, off, aux) __builtin_amdgcn_raw_buffer_load_b64((r), (off), 0, (aux))
#define ST128(v, r, off) __builtin_amdgcn_raw_buffer_store_b128((v), (r), (off), 0, AUX_SC1)
#define ST64(v, r, off) __builtin_amdgcn_raw_buffer_store_b64((v), (r), (off), 0, AUX_SC1)
#define ST32(v, r, off) __builtin_amdgcn_raw_buffer_store_b32((v), (r), (off), 0, AUX_SC1)

#define EPOCH_BYTES(e) [t0] "v"((e) & 0xFFu), [t1] "v"(((e) >> 8) & 0xFFu), [t2] "v"(((e) >> 16) & 0xFFu), [t3] "v"((e) >> 24)
#define P_XOR(a, b, c, d) "v_xor_b32 v" #a ", %[t0], v" #a "\nv_xor_b32 v" #b ", %[t1], v" #b "\nv_xor_b32 v" #c ", %[t2], v" #c "\nv_xor_b32 v" #d ", %[t3], v" #d "\n"
#define P_LOAD(n, a, b, c, d) "buffer_load_dwordx4 v[" #a ":" #d "], %[o" #n "], %[rs], 0 offen sc1\n"
__device__ __forceinline__ void poll1(v4u rs, unsigned e, v4u& g, int o) {
  unsigned x, cnt;
  asm volatile("s_mov_b32 %[cnt], 0x20000\n"
               "2:\ns_waitcnt vmcnt(0)\n" P_XOR(200, 201, 202, 203)
               "v_or3_b32 %[x], v200, v201, v202\nv_bitop3_b32 %[x], %[x], v203, %[ff] bitop3:0xa8\nv_cmp_ne_u32_e32 vcc, 0, %[x]\n"
               "s_cbranch_vccz 9f\n" P_LOAD(0, 200, 201, 202, 203)
               "s_sub_u32 %[cnt], %[cnt], 1\ns_cbranch_scc0 2b\ns_waitcnt vmcnt(0)\n9:\n"
               : "+{v[200:203]}"(g), [x] "=&v"(x), [cnt] "=&s"(cnt)
               : [o0] "v"(o), [rs] "s"(rs), EPOCH_BYTES(e), [ff] "v"(0xFFu)
               : "vcc", "scc", "memory");
}
#define P_CLEAN(n, a, b, c, d) P_XOR(a, b, c, d) "v_or3_b32 %[acc], v" #a ", v" #b ", %[acc]\nv_or3_b32 %[acc], v" #c ", v" #d ", %[acc]\n"
#define P_TEST(dst, d) "v_cmp_ne_u32_sdwa " dst ", v" #d ", %[t3] src0_sel:BYTE_0 src1_sel:DWORD\n"
#define P_TIGHT(n, a, b, c, d) "s_mov_b64 exec, %[m" #n "]\ns_cbranch_execz 3f\ns_branch 2f\n6:\n" P_LOAD(n, a, b, c, d) \
    "s_sub_u32 %[cnt], %[cnt], 1\ns_cbranch_scc1 8f\n2:\ns_waitcnt vmcnt(0)\n" P_TEST("vcc", d) "s_cbranch_vccnz 6b\ns_mov_b64 exec, -1\n" \
    P_CLEAN(n, a, b, c, d) "s_branch 4f\n3:\n"
#define P_OPEN(n, a, b, c, d) "s_mov_b64 %[m" #n "], -1\n"
#define P_CMP(n, a, b, c, d) P_TEST("%[c" #n "]", d)
#define P_MASK(n, a, b, c, d) "s_wqm_b64 %[c" #n "], %[c" #n "]\ns_and_b64 %[m" #n "], %[m" #n "], %[c" #n "]\n"
#define P_AGAIN(n, a, b, c, d) "s_mov_b64 exec, %[m" #n "]\ns_cbranch_execz 1f\n" P_LOAD(n, a, b, c, d) "1:\n"
#define P_CLOSE(n, a, b, c, d) "s_cmp_eq_u64 %[m" #n "], 0\ns_cbranch_scc0 1f\ns_bitcmp1_b32 %[raw], " #n "\ns_cbranch_scc0 1f\n" \
    "s_bitset0_b32 %[raw], " #n "\n" P_CLEAN(n, a, b, c, d) "1:\n"
#define P_OUT(n, r0, r1, r2, r3) "+{v[" #r0 ":" #r3 "]}"(g[n]), [m##n] "=&s"(m[n]), [c##n] "=&s"(cm[n]),
#define P_IN(n, a, b, c, d) [o##n] "v"(o[n]),
#define SLOTS4(M) M(0, 200, 201, 202, 203) M(1, 204, 205, 206, 207) M(2, 208, 209, 210, 211) M(3, 212, 213, 214, 215)
#define SLOTS6(M) SLOTS4(M) M(4, 216, 217, 218, 219) M(5, 220, 221, 222, 223)
#define ANY4 "s_or_b64 vcc, %[m0], %[m1]\ns_or_b64 vcc, vcc, %[m2]\ns_or_b64 vcc, vcc, %[m3]\n"
#define ANY6 ANY4 "s_or_b64 vcc, vcc, %[m4]\ns_or_b64 vcc, vcc, %[m5]\n"
#define POLL_N(NAME, N, SLOTS, ANY, ALL) \
__device__ __forceinline__ void NAME(v4u rs, unsigned e, v4u (&g)[N], const int (&o)[N]) { \
  u64 m[N], cm[N]; \
  unsigned acc, raw, cnt; \
  asm volatile("s_mov_b32 %[cnt], 0x20000\ns_branch 7f\n5:\n" SLOTS(P_TIGHT) \
               "10:\ns_sub_u32 %[cnt], %[cnt], 1\ns_cbranch_scc1 9f\n" SLOTS(P_LOAD) \
               "7:\n" SLOTS(P_OPEN) "s_mov_b32 %[raw], " ALL "\nv_mov_b32 %[acc], 0\n" \
               "0:\ns_waitcnt vmcnt(0)\n" SLOTS(P_CMP) SLOTS(P_MASK) ANY "s_cbranch_scc0 11f\n" SLOTS(P_AGAIN) \
               "s_mov_b64 exec, -1\ns_sub_u32 %[cnt], %[cnt], 1\ns_cbranch_scc1 8f\n11:\n" SLOTS(P_CLOSE) \
               "s_bcnt1_i32_b32 vcc_lo, %[raw]\ns_cbranch_scc0 4f\ns_cmp_eq_u32 vcc_lo, 1\ns_cbranch_scc1 5b\ns_branch 0b\n" \
               "8:\ns_mov_b64 exec, -1\ns_waitcnt vmcnt(0)\ns_branch 9f\n" \
               "4:\nv_cmp_ne_u32_sdwa vcc, %[acc], %[z] src0_sel:BYTE_0 src1_sel:DWORD\ns_cbranch_vccnz 10b\n9:\n" \
               : SLOTS(P_OUT) [acc] "=&v"(acc), [raw] "=&s"(raw), [cnt] "=&s"(cnt) \
               : SLOTS(P_IN) [rs] "s"(rs), EPOCH_BYTES(e), [z] "v"(0u) \
               : "vcc", "scc", "memory"); \
}
POLL_N(poll4, 4, SLOTS4, ANY4, "15")
POLL_N(poll6, 6, SLOTS6, ANY6, "63")
__device__ __forceinline__ f32x4 asf(v4u v) { return __builtin_bit_cast(f32x4, v); }
__device__ __forceinline__ f32x2 stats64(const float (&h)[4]) {
  float mu = ((h[0] + h[1]) + (h[2] + h[3])) * 0.25f, m2 = 0.f;
#pragma unroll
  for (int k = 0; k < 4; ++k) { const float d = h[k] - mu; m2 = fmaf(d, d, m2); }
  { const float o = dppf<XOR1>(mu), d = o - mu; m2 = fmaf(d * d, 2.0f, m2 + dppf<XOR1>(m2)); mu = (mu + o) * 0.5f; }
  { const float o = dppf<XOR2>(mu), d = o - mu; m2 = fmaf(d * d, 4.0f, m2 + dppf<XOR2>(m2)); mu = (mu + o) * 0.5f; }
  { const f32x2 a = swap16(mu), q = swap16(m2); const float d = a.x - a.y; m2 = fmaf(d * d, 8.0f, q.x + q.y); mu = (a.x + a.y) * 0.5f; }
  { const f32x2 a = swap32(mu), q = swap32(m2); const float d = a.x - a.y; m2 = fmaf(d * d, 16.0f, q.x + q.y); mu = (a.x + a.y) * 0.5f; }
  return f32x2{mu, m2};
}
__device__ __forceinline__ f32x2 stats_finish(LAS unsigned char* rec) {
  f32x4 st[4];
#pragma unroll
  for (int n = 0; n < 4; ++n) st[n] = *(LAS f32x4*)(rec + n * 16);
  const float mean = (((st[0].x + st[0].z) + (st[1].x + st[1].z)) + ((st[2].x + st[2].z) + (st[3].x + st[3].z))) * 0.125f;
  float q1 = 0.f, q2 = 0.f;
#pragma unroll
  for (int n = 0; n < 4; ++n) {
    const float d0 = st[n].x - mean, d1 = st[n].z - mean;
    q1 = fmaf(d0, d0, q1); q1 = fmaf(d1, d1, q1); q2 += st[n].y + st[n].w;
  }
  const float var = fmaf(q1, 64.0f, q2) * (1.0f / DM);
  return f32x2{rbf(mean), __builtin_amdgcn_rcpf(rbf(__builtin_amdgcn_sqrtf(rbf(rbf(var) + 1e-5f))))};
}
__device__ __forceinline__ void lds_arrive(LAS unsigned char* p, int lane) {
  asm volatile("s_waitcnt lgkmcnt(0)" ::: "memory");
  if (lane == 0) __hip_atomic_fetch_add((LAS unsigned*)p, 1u, __ATOMIC_RELAXED, __HIP_MEMORY_SCOPE_WORKGROUP);
}
__device__ __forceinline__ void lds_wait_ge(volatile LAS unsigned* f, unsigned v) {
#pragma unroll 1
  for (int spin = 0; spin < (SPIN_CAP << 4); ++spin) {
    if (*f >= v) break;
    __builtin_amdgcn_s_sleep(1);
  }
  CFENCE();
}

__device__ __forceinline__ float fold_half(f32x4 lo, f32x4 hi, bool low) {
  float a = lo.x + dppf<ROR8>(lo.y), b = hi.y + dppf<ROR8>(hi.x);
  asm("" : "+v"(a), "+v"(b));
  return low ? a : b;
}
#define FOLD_STORE(acc, e, off) do { \
    const v4u o_ = {mark_k<0>(fold_half(acc[0], acc[4], c < 8), e), mark_k<1>(fold_half(acc[1], acc[5], c < 8), e), \
                    mark_k<2>(fold_half(acc[2], acc[6], c < 8), e), mark_k<3>(fold_half(acc[3], acc[7], c < 8), e)}; \
    ST128(o_, rws, off); \
    PIN8(acc); } while (0)

__global__ void __launch_bounds__(512, 2)
fused_block(const u16*, const u16*, const u16*, const u16*, const u16*, const u16*, const u16*, const u16*, u16*, unsigned char*) {
  v16u ka;
  v4u kb;
  asm volatile("s_load_dwordx16 %0, %2, 0x0\n\ts_load_dwordx4 %1, %2, 0x40" : "=&s"(ka), "=&s"(kb) : "s"(__builtin_amdgcn_kernarg_segment_ptr()));
  extern __shared__ __attribute__((aligned(16))) unsigned char smem[];
  LAS unsigned char* L = (LAS unsigned char*)smem;
  const int tid = threadIdx.x, lane = tid & 63, w = __builtin_amdgcn_readfirstlane(tid >> 6);
  const int bid = blockIdx.x, g = bid & 7, idx = bid >> 3;
  const int G = lane >> 4, c = lane & 15;
  const f32x4 z4 = {0.f, 0.f, 0.f, 0.f};
  int zv = 0;
  asm volatile("" : "+v"(zv));
  u64 t_entry = __builtin_amdgcn_s_memrealtime();
  unsigned cv = 0u;
  if (idx < 8 && w == 0) {
    u64 pc0;
    asm volatile("s_getpc_b64 %0" : "=s"(pc0));
    cv = __builtin_amdgcn_raw_buffer_load_b32(MKRSRC(pc0 & ~255ull, CODE_BYTES), idx * 4096 + lane * 64, 0, 0);
  }
  const bool attn = idx >= 8;
  const int b = (idx - 8) / 6, s = (idx - 8) - b * 6, v = w & 3;
  const int nw = v < 3 ? 44 : (s < 4 ? 39 : 38);
  unsigned kvo = ((b * 8 + g) * SK + (170 * s + min(s, 4) + 44 * v) * 4) * (DH * 2);
  int kv_len = nw * 1024;
  int q_off = (w * 64 + (c >> 3) * 32 + G * 8) * (NQ * 2) + (g * 8 + idx) * 128 + (c & 7) * 16, l16 = lane * 16;
  int x_off0 = (c >> 2) * 1024 + (w * 64 + G * 16 + (c & 3) * 4) * 2, x_off1 = (c >> 2) * 1024 + (w * 64 + (c & 1) * 32 + G * 8) * 2;
  asm volatile("" : "+v"(q_off), "+v"(l16), "+v"(x_off0), "+v"(x_off1));
  asm volatile("s_waitcnt lgkmcnt(0)" : "+s"(ka), "+s"(kb), "+s"(t_entry), "+s"(kvo), "+s"(kv_len));
#define KARG(v, i) ((u64)(v)[2 * (i) + 1] << 32 | (v)[2 * (i)])
  const u16 *wo = (const u16*)KARG(ka, 4), *wg = (const u16*)KARG(ka, 5), *wu = (const u16*)KARG(ka, 6), *wd = (const u16*)KARG(ka, 7);
  u16* out = (u16*)KARG(kb, 0);
  unsigned char* ws = (unsigned char*)KARG(kb, 1);
  const __amdgpu_buffer_rsrc_t rx = MKRSRC(KARG(ka, 0), 4 * DM * 2);
  v4u R0[8], XA;
  v2u XR;
  if (!attn) {
    XR = LD64(rx, x_off0, 0);
    XA = LD128(rx, x_off1, 0);
    const __amdgpu_buffer_rsrc_t rq = MKRSRC(KARG(ka, 3), DM * NQ * 2);
#pragma unroll
    for (int n = 0; n < 8; ++n) R0[n] = __builtin_amdgcn_raw_buffer_load_b128(rq, q_off, n * (NQ * 2), AUX_NT);
  }
  const __amdgpu_buffer_rsrc_t rws = MKRSRC(ws, WS_END);
  const v4u sws = {kb[2], kb[3] & 0xFFFFu, (unsigned)WS_END, 0x00020000u};
  int role = idx;
  asm volatile("" : "+s"(role) :: "memory");

  if (__builtin_expect(role < 8, 0)) {
  const bool isO = idx < 4;
  const int f = idx < 7 ? bid - 32 : 24 + (g >> 1) + (g & 1) * 4;
  const bool hasW = f < NF;
  const GAS v4u* wp = (const GAS v4u*)(isO ? wo + (size_t)(g * 512 + idx * 128 + (c >> 3) * 64 + G * 16) * DM + w * 64 + (c & 7) * 8
                                           : (c < 8 ? wg : wu) + (size_t)(w * 64 + G * 16) * FF + f * 64 + (c & 7) * 8);
  const int wst = isO ? DM / 8 : FF / 8;
  v4u R[16];
  unsigned e1 = 0u, tag = 0u;
  {
    unsigned e0 = 0u;
    if (tid == 0) e0 = __builtin_amdgcn_raw_buffer_load_b32(rws, WS_E + zv, 0, AUX_SC1);
    SB();
    const float xs[4] = {bf_lo(XR.x), bf_hi(XR.x), bf_lo(XR.y), bf_hi(XR.y)};
    const f32x2 s_ = stats64(xs);
    if (G == 0 && (c & 3) == 0) *(LAS f32x2*)(L + L_ST + (c >> 2) * 64 + w * 8) = s_;
    LDS_BARRIER();
    const f32x2 ms = stats_finish(L + L_ST + (c >> 2) * 64);
    v4u A, bt[8];
#pragma unroll
    for (int k = 0; k < 4; ++k) A[k] = pack_bf(rbf(bf_lo(XA[k]) - ms.x) * ms.y, rbf(bf_hi(XA[k]) - ms.x) * ms.y);
    transpose8(R0, bt);
    asm volatile("" :: "v"(cv));
    f32x4 acc[8], o;
#pragma unroll
    for (int n = 0; n < 8; ++n) acc[n] = mfma32(A, bt[n], z4);
    PIN8(acc);
#pragma unroll
    for (int k = 0; k < 4; ++k) {
      const float send = c < 8 ? acc[k + 4].x : acc[k].y, keep = c < 8 ? acc[k].x : acc[k + 4].y;
      o[k] = keep + dppf<ROR8>(send);
    }
    *(LAS f32x4*)(L + L_QR + w * 1024 + G * 256 + (c & 7) * 32 + (c >> 3) * 16) = o;
    SB();
    LDS_BARRIER();
    if (w == 0) {
      e1 = __builtin_amdgcn_readfirstlane(e0) + EPOCH_STEP;
      tag = e1 * TAG_MUL;
      f32x4 a[8];
#pragma unroll
      for (int n = 0; n < 8; ++n) a[n] = *(LAS f32x4*)(L + L_QR + n * 1024 + lane * 16);
      const f32x4 t = SUM8(a);
      ST128((v4u{pack_bf(t.x, t.y), tag, pack_bf(t.z, t.w), tag}), rws, WS_Q + (G * 8 + g) * 2048 + idx * 256 + c * 16);
    }
  }
  const unsigned side = g >> 2;
#define LOAD_NEAR() LD64(rws, tid == 0 ? WS_NEAR : WS_END, AUX_SC1)
#define POST_NEAR(nw) if (w == 0) { \
      if (__builtin_amdgcn_readfirstlane(nw.y) != tag) { \
        _Pragma("unroll 1") for (int spin = 0; spin < SPIN_CAP; ++spin) { \
          CFENCE(); \
          nw = LOAD_NEAR(); \
          if (__builtin_amdgcn_readfirstlane(nw.y) == tag) break; } } \
      if (tid == 0) *(LAS v2u*)(L + L_NEAR) = v2u{((nw.x ^ side) & 1u) * DUP, e1}; }

  v4u W[16];
  if (hasW) {
    const unsigned blk = (unsigned)((uintptr_t)wp >> 7);
    const bool slow = !isO && __any((blk & 3u) == 3u);
    const int due = (isO ? ODELAY : FDELAY) - (slow ? SLOWADJ : 0);
    while ((int)(__builtin_amdgcn_s_memrealtime() - t_entry) < due) __builtin_amdgcn_s_sleep(4);
#pragma unroll
    for (int n = 0; n < 16; ++n) { PACE(); R[n] = __builtin_nontemporal_load(wp + n * wst); }
    SB();
  }

  if (isO) {
    const __amdgpu_buffer_rsrc_t rm = MKRSRC(ws + WS_ML + g * 1536 + idx * 16, w == 0 ? 1536 : 0);
    if (w == 0) while ((int)(__builtin_amdgcn_s_memrealtime() - t_entry) < MDELAY) __builtin_amdgcn_s_sleep(2);
    v4u ma = LD128(rm, lane * 64, AUX_SC1);
    v2u nw = LOAD_NEAR();
    SB();
    transpose8(R, W); PIN8(W);
    int po[6];
#pragma unroll
    for (int n = 0; n < 6; ++n) po[n] = p1_off((g * 4 + G) * 6 + n) + idx * 512 + w * 256 + c * 16;
    LAS unsigned char *a_w = L + L_O + G * 256 + (w & 1) * 128 + c * 8, *a_r = L + L_O + (c >> 2) * 256 + (c & 1) * 128 + G * 32;
    int o_p2 = WS_P2 + (g * 4 + idx) * 16384 + ((w >> 2) ^ side) * DUP + w * 1024 + G * 256 + (c & 7) * 32 + (c >> 3) * 16;
    asm volatile("" : "+v"(po[0]), "+v"(po[1]), "+v"(po[2]), "+v"(po[3]), "+v"(po[4]), "+v"(po[5]), "+v"(a_w), "+v"(a_r), "+v"(o_p2));
    if (w == 0) {
      if (lane >= 24) ma = v4u{0u, tag, 0u, tag};
#pragma unroll 1
      for (int spin = 0; spin < SPIN_CAP; ++spin) {
        const bool ba = ((ma.y ^ tag) | (ma.w ^ tag)) != 0u;
        if (!__any(ba)) break;
        __builtin_amdgcn_s_sleep(4);
        CFENCE();
        if (ba) ma = LD128(rm, lane * 64, AUX_SC1);
      }
      *(LAS v2u*)(L + L_ML + lane * 8) = v2u{ma.x, ma.z};
      POST_NEAR(nw);
    }
    transpose8(R + 8, W + 8); PIN8(W + 8);
    LDS_BARRIER();
    const int near = *(LAS int*)(L + L_NEAR);
    const unsigned ep = *(LAS unsigned*)(L + L_NEAR + 4);
    asm volatile("" :: "v"(ma), "v"(nw));
    o_p2 ^= near;
    if (w < 2) {
      v4u pg[6];
#pragma unroll
      for (int n = 0; n < 6; ++n) { po[n] += near; pg[n] = LD128(rws, po[n], AUX_SC1); }
      SB();
      float cf[6];
      f32x2 ml[6];
#pragma unroll
      for (int n = 0; n < 6; ++n) ml[n] = *(LAS f32x2*)(L + L_ML + (G * 6 + n) * 8);
      const float M = fmaxf(fmaxf(fmaxf(ml[0].x, ml[1].x), fmaxf(ml[2].x, ml[3].x)), fmaxf(ml[4].x, ml[5].x));
#pragma unroll
      for (int n = 0; n < 6; ++n) cf[n] = ml[n].y * __builtin_amdgcn_exp2f((ml[n].x - M) * LOG2E);
      const float r = __builtin_amdgcn_rcpf(((cf[0] + cf[1]) + (cf[2] + cf[3])) + (cf[4] + cf[5]));
#pragma unroll
      for (int n = 0; n < 6; ++n) cf[n] *= r;
      asm volatile("" : "+v"(cf[0]), "+v"(cf[1]), "+v"(cf[2]), "+v"(cf[3]), "+v"(cf[4]), "+v"(cf[5]));
      poll6(sws, ep, pg, po);
      const f32x4 ov = (asf(pg[0]) * cf[0] + asf(pg[1]) * cf[1] + asf(pg[2]) * cf[2]) + (asf(pg[3]) * cf[3] + asf(pg[4]) * cf[4] + asf(pg[5]) * cf[5]);
      *(LAS v2u*)(a_w) = v2u{pack_bf(ov.x, ov.y), pack_bf(ov.z, ov.w)};
    }
    LDS_BARRIER();
    {
      const v4u A0 = *(LAS v4u*)(a_r), A1 = *(LAS v4u*)(a_r + 16);
      f32x4 acc[8];
#pragma unroll
      for (int n = 0; n < 8; ++n) acc[n] = mfma32(A0, W[n], z4);
#pragma unroll
      for (int k = 0; k < 4; ++k) { acc[k] = mfma32(A1, W[8 + k], acc[k]); acc[k + 4] = mfma32(A1, W[12 + k], acc[k + 4]); }
      SB();
      FOLD_STORE(acc, ep, o_p2);
    }
  } else if (hasW) {
    v4u WD[8], R2[8];
    f32x2 cs;
    int near;
    unsigned ep;
    {
      v2u nw = LOAD_NEAR();
      const __amdgpu_buffer_rsrc_t rd = MKRSRC(wd, FF * DM * 2);
#pragma unroll
      for (int n = 0; n < 8; ++n) { PACE(); R2[n] = LD128(rd, (f * 64 + (c >> 3) * 32 + G * 8 + n) * (DM * 2) + w * 128 + (c & 7) * 16, AUX_NT); }
      transpose8(R, W); PIN8(W);
      transpose8(R + 8, W + 8); PIN8(W + 8);
      const v4u ones = {0x3F803F80u, 0x3F803F80u, 0x3F803F80u, 0x3F803F80u};
      f32x4 o0, o1;
#pragma unroll
      for (int n = 0; n < 4; ++n) {
        o0[n] = mfma32(ones, W[8 + n], mfma32(ones, W[n], z4)).x;
        o1[n] = mfma32(ones, W[12 + n], mfma32(ones, W[4 + n], z4)).x;
      }
      if (G == 0) {
        LAS unsigned char* cr = L + L_CS + w * 512 + c * 32;
        *(LAS f32x4*)(cr) = o0; *(LAS f32x4*)(cr + 16) = o1;
      }
      POST_NEAR(nw);
      LDS_BARRIER();
      near = *(LAS int*)(L + L_NEAR);
      ep = *(LAS unsigned*)(L + L_NEAR + 4);
      f32x2 a[8];
#pragma unroll
      for (int n = 0; n < 8; ++n) a[n] = f32x2{*(LAS float*)(L + L_CS + n * 512 + lane * 4), *(LAS float*)(L + L_CS + n * 512 + 256 + lane * 4)};
      cs = SUM8(a);
      asm volatile("" : "+v"(cs));
      SB();
    }
    LAS unsigned char *a_st = L + L_ST2 + (c >> 2) * 64 + w * 8 + (G & 1) * 4, *a_gw = L + L_GR + w * 2048 + G * 512 + (c >> 2) * 128 + (c & 4) * 16 + (c & 3) * 16;
    LAS unsigned char* a_gx = L + L_GR + w * 2048 + G * 512 + (c >> 2) * 128 + ((c & 4) ^ 4) * 16 + (c & 3) * 16;
    LAS unsigned char *a_sr = L + L_ST2 + (w & 3) * 64 + (lane & 7) * 8, *a_aw = L + L_ACT + (w & 3) * 128 + lane * 2;
    LAS unsigned char* a_r = L + L_GR + (w & 3) * 512 + (lane >> 5) * 128 + (((lane >> 2) ^ (lane >> 5)) & 1) * 64 + ((lane >> 3) & 3) * 16 + (lane & 3) * 4;
    LAS unsigned char* a_ar = L + L_ACT + (c >> 2) * 128 + (c & 1) * 64 + G * 16;
    int o_p3 = WS_P3 + f * 16384 + (near ^ (((w >> 2) ^ side) * DUP)) + w * 1024 + G * 256 + (c & 7) * 32 + (c >> 3) * 16;
    const int ho = WS_H + near + w * 1024 + (c >> 2) * 256 + G * 64 + (c & 3) * 16;
    asm volatile("" : "+v"(a_st), "+v"(a_gw), "+v"(a_gx), "+v"(a_sr), "+v"(a_r), "+v"(a_aw), "+v"(a_ar), "+v"(o_p3));
    SB();
    transpose8(R2, WD); PIN8(WD);
    v4u hg = LD128(rws, ho, AUX_SC1);
    poll1(sws, ep, hg, ho);
    {
      const unsigned h0 = perm_hi(hg.x, hg.y), h1 = perm_hi(hg.z, hg.w);
      const v4u A0 = {dppu<BC0>(h0), dppu<BC0>(h1), dppu<BC1>(h0), dppu<BC1>(h1)};
      const v4u A1 = {dppu<BC2>(h0), dppu<BC2>(h1), dppu<BC3>(h0), dppu<BC3>(h1)};
      SB();
      f32x4 acc[8];
#pragma unroll
      for (int hh = 0; hh < 2; ++hh) {
#pragma unroll
        for (int n = 4 * hh; n < 4 * hh + 4; ++n) acc[n] = mfma32(A0, W[n], z4);
#pragma unroll
        for (int n = 4 * hh; n < 4 * hh + 4; ++n) acc[n] = mfma32(A1, W[8 + n], acc[n]);
      }
      float s1 = dot2(h1, 0x3F803F80u, dot2(h0, 0x3F803F80u, 0.f)), s2 = dot2(h1, h1, dot2(h0, h0, 0.f));
      s1 += dppf<XOR1>(s1); s2 += dppf<XOR1>(s2);
      s1 += dppf<XOR2>(s1); s2 += dppf<XOR2>(s2);
      const auto e = __builtin_amdgcn_permlane16_swap(f2u(s1), f2u(s2), false, false);
      const f32x2 t = swap32(u2f(e[0]) + u2f(e[1]));
      *(LAS float*)(a_st) = t.x + t.y;
#pragma unroll
      for (int n = 0; n < 16; ++n) { __builtin_amdgcn_sched_group_barrier(0x8, 1, 0); __builtin_amdgcn_sched_group_barrier(0x2, 1, 0); }
      SB();
      PIN4(acc);
      *(LAS f32x4*)(a_gw) = f32x4{acc[0].x, acc[1].x, acc[2].x, acc[3].x};
      PIN4(acc + 4);
      *(LAS f32x4*)(a_gx) = f32x4{acc[4].x, acc[5].x, acc[6].x, acc[7].x};
    }
    LDS_BARRIER();
    if (w < 4) {
      f32x2 st = *(LAS f32x2*)(a_sr);
      f32x2 a[8];
#pragma unroll
      for (int n = 0; n < 8; ++n) a[n] = f32x2{*(LAS float*)(a_r + n * 2048), *(LAS float*)(a_r + n * 2048 + 256)};
      SB();
      st.x += dppf<XOR1>(st.x); st.y += dppf<XOR1>(st.y);
      st.x += dppf<XOR2>(st.x); st.y += dppf<XOR2>(st.y);
      st.x += dppf<HMIR>(st.x); st.y += dppf<HMIR>(st.y);
      const float mean = st.x * (1.0f / DM), var = fmaxf(fmaf(-mean, mean, st.y * (1.0f / DM)), 0.f);
      const float rs = __builtin_amdgcn_rcpf(rbf(__builtin_amdgcn_sqrtf(rbf(rbf(var) + 1e-5f))));
      const f32x2 gu = (SUM8(a) - cs * rbf(mean)) * rs;
      unsigned gp = pack_bf(gu.x, gu.y);
      asm("" : "+v"(gp));
      const float gg = bf_lo(gp);
      const float sg = rbf(gg * __builtin_amdgcn_rcpf(1.0f + __builtin_amdgcn_exp2f(-gg * LOG2E)));
      *(LAS u16*)(a_aw) = (u16)pack_bf(sg * bf_hi(gp), 0.f);
    }
    LDS_BARRIER();
    const v4u A = *(LAS v4u*)(a_ar);
    f32x4 acc[8];
#pragma unroll
    for (int k = 0; k < 4; ++k) { acc[k] = mfma32(A, WD[k], z4); acc[k + 4] = mfma32(A, WD[k + 4], z4); }
    SB();
    FOLD_STORE(acc, ep, o_p3);
  } else if (f == 31) {
    volatile LAS int* vote = (volatile LAS int*)(L + L_ST2);
    int mn0 = 1 << 20, mn1 = 1 << 20;
#pragma unroll 1
    for (int n = 0; n < 6; ++n) {
      const u64 t0 = __builtin_amdgcn_s_memtime();
      asm volatile("s_waitcnt lgkmcnt(0)" ::: "memory");
      ST32(0u, rws, WS_PROBE + (n & 1) * DUP + w * 1024 + (n >> 1) * 256 + (lane & 31) * 4);
      asm volatile("s_waitcnt vmcnt(0)" ::: "memory");
      const int dt = (int)(__builtin_amdgcn_s_memtime() - t0);
      if (n & 1) mn1 = min(mn1, dt); else mn0 = min(mn0, dt);
    }
    vote[w] = mn0; vote[8 + w] = mn1;
    LDS_BARRIER();
    if (tid == 0) {
#pragma unroll
      for (int n = 1; n < 8; ++n) { mn0 = min(mn0, vote[n]); mn1 = min(mn1, vote[8 + n]); }
      ST64((v2u{(mn0 > mn1 ? 1u : 0u) ^ side, tag}), rws, WS_NEAR);
    }
  }
  }
  asm volatile("" : "+s"(role) :: "memory");
  if (__builtin_expect(role >= 8, 1)) {
    const int slot = (g * 4 + b) * 6 + s;
    volatile LAS unsigned* flag = (volatile LAS unsigned*)(L + L_FLAG);
    LAS unsigned char* a_r;
    int o_1;
    unsigned t8;
#define A_END_ADDR() do { \
      t8 = (flag[3] >> (8 * (tid & 3))) & 0xFFu; \
      a_r = L + L_RED + (tid >> 7) * 640 + ((tid >> 2) & 1) * 320 + ((tid >> 3) & 15) * 16 + (tid & 3) * 4; \
      o_1 = p1_off(slot) + tid * 4; \
      asm volatile("" : "+v"(a_r), "+v"(o_1), "+v"(t8)); } while (0)
    int wr = w;
    if (__builtin_expect(w >= 4, 0)) {
      const __amdgpu_buffer_rsrc_t rv = MKRSRC(KARG(ka, 2) + kvo, kv_len);
      if (tid < 260) flag[tid & 3] = 0u;
      LDS_BARRIER();
      v4u V[44];
#pragma unroll
      for (int u = 0; u < VSPLIT; ++u) V[u] = LD128(rv, u * 1024 + lane * 16, AUX_NT);
      SB();
      lds_wait_ge(flag + 2, 4u);
      LAS unsigned char* pp = L + L_P + (((c >> 2) * 4 + G) * NUP + 48 * v) * 2;
      const v2u pl = *(LAS v2u*)(pp + 80);
      f32x4 acc[8];
#pragma unroll
      for (int T = 0; T < 5; ++T) {
        if (T >= 1 && T <= 3) {
#pragma unroll
          for (int u = VSPLIT + (44 - VSPLIT) * (T - 1) / 3; u < VSPLIT + (44 - VSPLIT) * T / 3; ++u) V[u] = LD128(rv, u * 1024 + lane * 16, AUX_NT);
          SB();
        }
        const v4u pf = *(LAS v4u*)(pp + T * 16);
        v4u bt[8];
        transpose8(V + 8 * T, bt);
#pragma unroll
        for (int n = 0; n < 8; ++n) acc[n] = mfma32(pf, bt[n], T ? acc[n] : z4);
        SB();
      }
      A_END_ADDR();
      LAS unsigned char* a_w = L + L_RED + v * 2560 + G * 640 + c * 16;
      asm volatile("" : "+v"(a_w));
      unsigned t0[8];
#pragma unroll
      for (int d2 = 0; d2 < 4; ++d2) { t0[2 * d2] = perm_lo(V[40][d2], V[41][d2]); t0[2 * d2 + 1] = perm_hi(V[40][d2], V[41][d2]); }
      SB();
#pragma unroll
      for (int d2 = 0; d2 < 4; ++d2) {
        acc[2 * d2] = mfma16(pl, v2u{t0[2 * d2], perm_lo(V[42][d2], V[43][d2])}, acc[2 * d2]);
        acc[2 * d2 + 1] = mfma16(pl, v2u{t0[2 * d2 + 1], perm_hi(V[42][d2], V[43][d2])}, acc[2 * d2 + 1]);
      }
      SB();
      PIN4(acc);
      *(LAS f32x4*)(a_w) = f32x4{acc[0].x, acc[1].x, acc[2].x, acc[3].x};
      PIN4(acc + 4);
      *(LAS f32x4*)(a_w + 320) = f32x4{acc[4].x, acc[5].x, acc[6].x, acc[7].x};
    }
    asm volatile("" : "+s"(wr) :: "memory");
    if (__builtin_expect(wr < 4, 1)) {
      const __amdgpu_buffer_rsrc_t rk = MKRSRC(KARG(ka, 1) + kvo, kv_len);
      v4u K[44];
#pragma unroll
      for (int u = 0; u < 44; ++u) {
        K[u] = LD128(rk, u * 1024 + l16, AUX_NT);
        if (u == KBAR - 1) { SB(); __builtin_amdgcn_s_barrier(); SB(); }
      }
      const __amdgpu_buffer_rsrc_t rq0 = MKRSRC(ws, w == 0 ? WS_END : 0);
      const int qo = WS_Q + (b * 8 + g) * 2048 + lane * 16;
      const unsigned e0 = __builtin_amdgcn_raw_buffer_load_b32(rq0, lane == 0 ? WS_E : WS_END, 0, AUX_SC1);
      v4u q0, q1;
      SB();
      LAS unsigned char* KS = L + L_KS + w * KW;
      int ko[4];
#pragma unroll
      for (int r = 0; r < 4; ++r) ko[r] = G * 256 + ((c ^ (4 * r + G)) * 16);
#pragma unroll
      for (int u = 0; u < 36; ++u) {
        *(LAS v4u*)(KS + u * 1024 + ko[u & 3]) = K[u];
        if (u == QPOS - 1) { SB(); q0 = LD128(rq0, qo, AUX_SC1); q1 = LD128(rq0, qo + 1024, AUX_SC1); SB(); }
      }
      CFENCE();
      v4u kf[44];
      LAS unsigned char* kp = KS + c * 256;
#pragma unroll
      for (int n = 0; n < 36; ++n) kf[n] = *(LAS v4u*)(kp + (n >> 2) * 4096 + (((4 * (n & 3) + G) ^ c) * 16));
      CFENCE();
#pragma unroll
      for (int u = 36; u < 44; ++u) *(LAS v4u*)(KS + (u - 36) * 1024 + ko[u & 3]) = K[u];
      CFENCE();
#pragma unroll
      for (int n = 36; n < 44; ++n) kf[n] = *(LAS v4u*)(kp + ((n >> 2) - 9) * 4096 + (((4 * (n & 3) + G) ^ c) * 16));
      SB();
      const unsigned ea = __builtin_amdgcn_readfirstlane(e0) + EPOCH_STEP, tag = ea * TAG_MUL;
      if (w == 0) {
        flag[3] = ea;
#pragma unroll 1
        for (int spin = 0; spin < SPIN_CAP; ++spin) {
          if (!__any(((q0.y ^ tag) | (q0.w ^ tag) | (q1.y ^ tag) | (q1.w ^ tag)) != 0u)) break;
          CFENCE();
          q0 = LD128(rws, qo, AUX_SC1); q1 = LD128(rws, qo + 1024, AUX_SC1);
        }
        *(LAS v2u*)(L + L_QS + lane * 8) = v2u{q0.x, q0.z};
        *(LAS v2u*)(L + L_QS + 512 + lane * 8) = v2u{q1.x, q1.z};
        lds_arrive(L + L_FLAG, lane);
      }
      lds_wait_ge(flag, 1u);
      f32x4 sc[3] = {z4, z4, z4};
      v4u qf[4];
#pragma unroll
      for (int ks = 0; ks < 4; ++ks) qf[ks] = *(LAS v4u*)(L + L_QS + (c & 3) * 256 + ks * 64 + G * 16);
#pragma unroll
      for (int tv = 0; tv < 4; ++tv)
#pragma unroll
        for (int ks = 0; ks < 4; ++ks) {
          const v4u qz = (c >> 2) == tv ? qf[ks] : v4u{0u, 0u, 0u, 0u};
#pragma unroll
          for (int a = 0; a < 3; ++a)
            if (4 * a + tv < 11) sc[a] = mfma32(qz, kf[(4 * a + tv) * 4 + ks], sc[a]);
        }
      const bool live = 128 + lane < 4 * nw;
      const bool b0 = lane & 1, b1 = lane & 2;
      float t[4];
#pragma unroll
      for (int r = 0; r < 4; ++r) {
#pragma unroll
        for (int a = 0; a < 3; ++a) sc[a][r] = rbf(rbf(sc[a][r]) * 0.08838834764831845f);
        if (!live) sc[2][r] = -INFINITY;
        t[r] = fmaxf(fmaxf(sc[0][r], sc[1][r]), sc[2][r]);
      }
      float mq;
      {
        const float u0 = fmaxf(b0 ? t[1] : t[0], dppf<XOR1>(b0 ? t[0] : t[1])), u1 = fmaxf(b0 ? t[3] : t[2], dppf<XOR1>(b0 ? t[2] : t[3]));
        mq = fmaxf(b1 ? u1 : u0, dppf<XOR2>(b1 ? u0 : u1));
        mq = fmaxf(mq, dppf<ROR4>(mq)); mq = fmaxf(mq, dppf<ROR8>(mq)); mq = rows_max(mq);
      }
      const float m[4] = {dppf<BC0>(mq), dppf<BC1>(mq), dppf<BC2>(mq), dppf<BC3>(mq)};
#pragma unroll
      for (int r = 0; r < 4; ++r) {
#pragma unroll
        for (int a = 0; a < 3; ++a) sc[a][r] = __builtin_amdgcn_exp2f((sc[a][r] - m[r]) * LOG2E);
        t[r] = (sc[0][r] + sc[1][r]) + sc[2][r];
      }
      float lq;
      {
        const float u0 = (b0 ? t[1] : t[0]) + dppf<XOR1>(b0 ? t[0] : t[1]), u1 = (b0 ? t[3] : t[2]) + dppf<XOR1>(b0 ? t[2] : t[3]);
        lq = (b1 ? u1 : u0) + dppf<XOR2>(b1 ? u0 : u1);
        lq += dppf<ROR8>(lq); lq += dppf<ROR4>(lq); lq = rows_sum(lq);
      }
      if (lane < 4) *(LAS f32x2*)(L + L_MLW + lane * 32 + w * 8) = f32x2{mq, lq};
      lds_arrive(L + L_FLAG + 4, lane);
      lds_wait_ge(flag + 1, 4u);
      const f32x4 r0 = *(LAS f32x4*)(L + L_MLW + (lane & 3) * 32), r1 = *(LAS f32x4*)(L + L_MLW + (lane & 3) * 32 + 16);
      const float M = fmaxf(fmaxf(r0.x, r0.z), fmaxf(r1.x, r1.z));
      const float Ls = fmaf(r0.y, __builtin_amdgcn_exp2f((r0.x - M) * LOG2E), r0.w * __builtin_amdgcn_exp2f((r0.z - M) * LOG2E)) +
                       fmaf(r1.y, __builtin_amdgcn_exp2f((r1.x - M) * LOG2E), r1.w * __builtin_amdgcn_exp2f((r1.z - M) * LOG2E));
      const float fq = __builtin_amdgcn_exp2f((mq - M) * LOG2E) * __builtin_amdgcn_rcpf(Ls);
      const float fac[4] = {dppf<BC0>(fq), dppf<BC1>(fq), dppf<BC2>(fq), dppf<BC3>(fq)};
      LAS unsigned char* pp = L + L_P + ((c & 3) * NUP + 48 * w + 4 * G + (c >> 2)) * 2;
#pragma unroll
      for (int r = 0; r < 4; ++r)
#pragma unroll
        for (int a = 0; a < 3; ++a) *(LAS u16*)(pp + (r * 4 * NUP + 16 * a) * 2) = (u16)pack_bf(sc[a][r] * fac[r], 0.f);
      const v4u mlo = {f2u(M), tag, f2u(Ls), tag};
      lds_arrive(L + L_FLAG + 8, lane);
      if (tid < 4) ST128(mlo, rws, WS_ML + slot * 64 + tid * 16);
      if (w == 0) {
        v2u nv;
#pragma unroll 1
        for (int spin = 0; spin < SPIN_CAP; ++spin) {
          nv = LD64(rws, lane == 0 ? WS_NEAR : WS_END, AUX_SC1);
          CFENCE();
          if (__builtin_amdgcn_readfirstlane(nv.y) == tag) break;
        }
        if (lane == 0) flag[4] = ((nv.x ^ (g >> 2)) & 1u) * DUP;
      }
      A_END_ADDR();
    }
    LDS_BARRIER();
    {
      float a[4];
#pragma unroll
      for (int n = 0; n < 4; ++n) a[n] = *(LAS float*)(a_r + n * 2560);
      const int near = flag[4];
      const unsigned o = mark((a[0] + a[2]) + (a[1] + a[3]), t8);
      ST32(o, rws, o_1 + near);
    }
    if (s == RS) {
    const int near = flag[4];
    const unsigned ep = flag[3];
    const int q = (G & 1) * 2 + (G >> 1);
#define FOLD4(v, y) \
      const auto y##a = __builtin_amdgcn_permlane16_swap(v[0], v[2], false, false), y##b = __builtin_amdgcn_permlane16_swap(v[1], v[3], false, false); \
      const auto y##c = __builtin_amdgcn_permlane32_swap(f2u(u2f(y##a[0]) + u2f(y##a[1])), f2u(u2f(y##b[0]) + u2f(y##b[1])), false, false); \
      float y = u2f(y##c[0]) + u2f(y##c[1]); \
      y += dppf<ROR8>(y);
    if (w < 2) {
      const int col = w * 32 + (lane & 7) * 4 + q;
      v4u hg[4];
      int ho[4];
#pragma unroll
      for (int n = 0; n < 4; ++n) { ho[n] = WS_P2 + near + (8 * n + (lane >> 3)) * 16384 + g * 1024 + b * 256 + w * 128 + (lane & 7) * 16; hg[n] = LD128(rws, ho[n], AUX_SC1); }
      float xq = u2f((unsigned)__builtin_amdgcn_raw_buffer_load_b16(rx, b * 1024 + (g * 64 + col) * 2, 0, 0) << 16);
      int o_far = c < 8 ? WS_H + (near ^ DUP) + g * 1024 + b * 256 + col * 4 : WS_END, o_near = c < 8 ? o_far ^ DUP : WS_END;
      unsigned tq = (ep >> (8 * q)) & 0xFFu;
      asm volatile("" : "+v"(xq), "+v"(o_far), "+v"(o_near), "+v"(tq));
      poll4(sws, ep, hg, ho);
      const v4u hs = __builtin_bit_cast(v4u, (asf(hg[0]) + asf(hg[1])) + (asf(hg[2]) + asf(hg[3])));
      FOLD4(hs, ha)
      const unsigned hb = pack_bf(0.f, xq + rbf(ha));
      ST32(hb | tq, rws, o_far);
      ST32(hb | tq, rws, o_near);
      *(LAS unsigned*)(L + L_QS + col * 4) = hb;
      if (slot == RS && tid == 0) ST32(ep, rws, WS_E);
    }
    LDS_BARRIER();
    {
      const int col = w * 8 + (lane & 1) * 4 + q;
      float hq = *(LAS float*)(L + L_QS + col * 4);
      int oo = c < 2 ? (b * DM + g * 64 + col) * 2 : 4 * DM * 2;
      const int o3 = WS_P3 + near + (lane >> 1) * 16384 + g * 1024 + b * 256 + w * 32 + (lane & 1) * 16;
      asm volatile("" : "+v"(hq), "+v"(oo));
      v4u p3 = {0u, 0u, 0u, 0u};
      if (lane < 2 * NF) { p3 = LD128(rws, o3, AUX_SC1); poll1(sws, ep, p3, o3); }
      FOLD4(p3, y)
      y += dppf<ROR4>(y); y += dppf<XOR2>(y);
      __builtin_amdgcn_raw_buffer_store_b16((u16)pack_bf(hq + rbf(y), 0.f), MKRSRC(out, 4 * DM * 2), oo, 0, 0);
    }
    }
  }
}

extern "C" void kernel_launch(void* const* d_in, const int* in_sizes, int n_in, void* d_out, int out_size, void* d_ws, size_t ws_size,
                              hipStream_t stream) {
  (void)hipFuncSetAttribute((const void*)fused_block, hipFuncAttributeMaxDynamicSharedMemorySize, L_END);
  fused_block<<<256, 512, L_END, stream>>>((const u16*)d_in[0], (const u16*)d_in[1], (const u16*)d_in[2], (const u16*)d_in[3], (const u16*)d_in[4],
                                           (const u16*)d_in[6], (const u16*)d_in[7], (const u16*)d_in[8], (u16*)d_out,
                                           (unsigned char*)(((uintptr_t)d_ws + (2u << 20) - 1) & ~(uintptr_t)((2u << 20) - 1)));
}
```
